```python
import torch

def _softmax(x, axis=-1):
    return torch.softmax(x, dim=axis)
def _gelu(x):
    return torch.nn.functional.gelu(x, approximate="tanh")
def _silu(x):
    return torch.nn.functional.silu(x)
def _sigmoid(x):
    return torch.sigmoid(x)
def _layernorm(x, axis=-1, eps=1e-5):
    m = x.mean(dim=axis, keepdim=True)
    v = x.var(dim=axis, keepdim=True, unbiased=False)
    return (x - m) / torch.sqrt(v + eps)
def _scatter_add(tgt, idx, src, axis=0):
    return tgt.clone().index_add_(axis, idx, src)
def _segment_sum(src, seg, n):
    out = torch.zeros((n,) + src.shape[1:], dtype=src.dtype)
    return out.index_add_(0, seg, src)
def _pad(x, pw):
    flat = tuple(v for pair in reversed(pw) for v in pair)
    return torch.nn.functional.pad(x, flat)

kernel_name = 'concat_heads__gqa_block_cached__gqa_block_cached__decode'

def setup_inputs():

    _w = (lambda a: (lambda t: t.to(torch.bfloat16) if t.is_floating_point() else t)(torch.as_tensor(a)))
    def _A_setup():
        B, Sq, D = 4, 1, 448
        Hq, Hkv, Sk, Dh = 16, 8, 8192, 256
        assert Hq % Hkv == 0, (Hq, Hkv)
        return {
            "x": _w((lambda *s: torch.randn(*s, dtype=torch.bfloat16) * 0.8)(B, Sq, D)),
            "k_cache": _w((lambda *s: torch.randn(*s, dtype=torch.bfloat16) * 0.8)(B, Hkv, Sk, Dh)),
            "v_cache": _w((lambda *s: torch.randn(*s, dtype=torch.bfloat16) * 0.05)(B, Hkv, Sk, Dh)),
            "wq": _w((lambda *s: torch.randn(*s, dtype=torch.bfloat16) * 0.8)(D, Hq * Dh) * (D ** -0.5)),
            "wo": _w((lambda *s: torch.randn(*s, dtype=torch.bfloat16) * 0.05)(Hq * Dh, D) * ((Hq * Dh) ** -0.5)),
        }
    _A_in = _A_setup()

    def _B_setup():
        B, Sq, D = 4, 1, 448
        Hq, Hkv, Sk, Dh = 16, 8, 8192, 256
        assert Hq % Hkv == 0, (Hq, Hkv)
        return {
            "x": _w((lambda *s: torch.randn(*s, dtype=torch.bfloat16) * 0.8)(B, Sq, D)),
            "k_cache": _w((lambda *s: torch.randn(*s, dtype=torch.bfloat16) * 0.8)(B, Hkv, Sk, Dh)),
            "v_cache": _w((lambda *s: torch.randn(*s, dtype=torch.bfloat16) * 0.05)(B, Hkv, Sk, Dh)),
            "wq": _w((lambda *s: torch.randn(*s, dtype=torch.bfloat16) * 0.8)(D, Hq * Dh) * (D ** -0.5)),
            "wo": _w((lambda *s: torch.randn(*s, dtype=torch.bfloat16) * 0.05)(Hq * Dh, D) * ((Hq * Dh) ** -0.5)),
        }
    _B_in = _B_setup()

    return {
        "A_x": _A_in["x"],
        "A_k_cache": _A_in["k_cache"],
        "A_v_cache": _A_in["v_cache"],
        "A_wq": _A_in["wq"],
        "A_wo": _A_in["wo"],
        "B_x": _B_in["x"],
        "B_k_cache": _B_in["k_cache"],
        "B_v_cache": _B_in["v_cache"],
        "B_wq": _B_in["wq"],
        "B_wo": _B_in["wo"],
    }

def reference(A_x, A_k_cache, A_v_cache, A_wq, A_wo, B_x, B_k_cache, B_v_cache, B_wq, B_wo):
    def _A_ref(x, k_cache, v_cache, wq, wo):
        B, Sq, D = x.shape
        Hq, Dh = 16, 256
        Hkv = k_cache.shape[1]
        rep = Hq // Hkv
        q = (lambda x, *d: x.permute(*d))((x @ wq).reshape(B, Sq, Hq, Dh), 0, 2, 1, 3)
        k = (lambda x, n, axis: torch.repeat_interleave(x, n, dim=axis))(k_cache, rep, axis=1)
        v = (lambda x, n, axis: torch.repeat_interleave(x, n, dim=axis))(v_cache, rep, axis=1)
        scores = torch.einsum("bhqd,bhkd->bhqk", q, k) * (Dh ** -0.5)
        p = _softmax(scores, axis=-1)
        o = torch.einsum("bhqk,bhkd->bhqd", p, v)
        return (lambda x, *d: x.permute(*d))(o, 0, 2, 1, 3).reshape(B, Sq, Hq * Dh) @ wo

    def _B_ref(x, k_cache, v_cache, wq, wo):
        B, Sq, D = x.shape
        Hq, Dh = 16, 256
        Hkv = k_cache.shape[1]
        rep = Hq // Hkv
        q = (lambda x, *d: x.permute(*d))((x @ wq).reshape(B, Sq, Hq, Dh), 0, 2, 1, 3)
        k = (lambda x, n, axis: torch.repeat_interleave(x, n, dim=axis))(k_cache, rep, axis=1)
        v = (lambda x, n, axis: torch.repeat_interleave(x, n, dim=axis))(v_cache, rep, axis=1)
        scores = torch.einsum("bhqd,bhkd->bhqk", q, k) * (Dh ** -0.5)
        p = _softmax(scores, axis=-1)
        o = torch.einsum("bhqk,bhkd->bhqd", p, v)
        return (lambda x, *d: x.permute(*d))(o, 0, 2, 1, 3).reshape(B, Sq, Hq * Dh) @ wo

    a = _A_ref(A_x, A_k_cache, A_v_cache, A_wq, A_wo)
    b = _B_ref(B_x, B_k_cache, B_v_cache, B_wq, B_wo)
    return torch.concatenate([a, b], axis=-1)
```

```cpp
#include <hip/hip_runtime.h>
#include <hip/hip_bf16.h>

typedef unsigned u32;
typedef unsigned long long u64;
typedef u32 v4u __attribute__((ext_vector_type(4)));
typedef u32 v2u __attribute__((ext_vector_type(2)));
typedef float f4 __attribute__((ext_vector_type(4)));
typedef float f2 __attribute__((ext_vector_type(2)));
typedef short s4 __attribute__((ext_vector_type(4)));
typedef __bf16 bf8 __attribute__((ext_vector_type(8)));
typedef __bf16 bf2 __attribute__((ext_vector_type(2)));
#define LAS __attribute__((address_space(3)))
#define GAS __attribute__((address_space(1)))

#define DIAG 0
#define QDELAY 40

constexpr int DM = 448, NQ = 4096, SK = 8192;
constexpr int UNIT_BYTES = SK * 512;
constexpr int BATCH_BYTES = 8 * UNIT_BYTES;
constexpr int NT = 2048;
constexpr int NSTATIC = 256;
constexpr int AUX_NT = 2, AUX_SC1 = 16;
constexpr int RSRC3 = 0x00020000;
constexpr u32 TAG_STEP = 0x9E3779B9u;
constexpr u32 SPIN_MAX = 1u << 18;

constexpr int WS_EPOCH = 0;
constexpr int WS_CNT = 4096;
constexpr int WS_QB = 16384;
constexpr int WS_ML = WS_QB + 16 * 8192;
constexpr int WS_P1 = WS_ML + 16 * 1024;
constexpr int WS_P2 = WS_P1 + 16 * 4 * 8 * 4096;
constexpr int WS_END = WS_P2 + 2 * 2 * 64 * 7168;
constexpr int WS_DIAG = 8 << 20;

constexpr int WO_STRIDE = 464;
constexpr int L_WQ = 0;
constexpr int L_QP = 28672;
constexpr int L_QF = L_QP + 3584;
constexpr int L_WO = L_QF + 2048;
constexpr int L_REC = L_WO + 64 * WO_STRIDE;
constexpr int L_RML = L_REC + 32768;
constexpr int L_X = L_RML + 256;
constexpr int L_END = L_X + 512;
static_assert(L_END > 81920 && L_END <= 163840 && (L_WO % 16) == 0 && (L_REC % 16) == 0, "one workgroup per CU");

struct Branch { const void* x; const void* wq; const void* k; const void* v; const void* wo; void* out; void* ws; void* ws2; };
struct Params { Branch b[2]; };

__device__ __forceinline__ __amdgpu_buffer_rsrc_t mk(const void* p, int bytes) {
  return __builtin_amdgcn_make_buffer_rsrc((void*)p, 0, bytes, RSRC3);
}
__device__ __forceinline__ f4 mfma(v4u a, v4u b, f4 c) {
  return __builtin_amdgcn_mfma_f32_16x16x32_bf16(__builtin_bit_cast(bf8, a), __builtin_bit_cast(bf8, b), c, 0, 0, 0);
}
template <int CTRL> __device__ __forceinline__ float dppf(float x) {
  return __builtin_bit_cast(float, __builtin_amdgcn_mov_dpp(__builtin_bit_cast(int, x), CTRL, 0xf, 0xf, true));
}
template <int CTRL> __device__ __forceinline__ u32 dppu(u32 x) {
  return (u32)__builtin_amdgcn_mov_dpp((int)x, CTRL, 0xf, 0xf, true);
}
constexpr int HALF_MIRROR = 0x141, ROR4 = 0x124, ROR8 = 0x128;
__device__ __forceinline__ float x16_max(float x) {
  auto s = __builtin_amdgcn_permlane16_swap(__float_as_uint(x), __float_as_uint(x), false, false);
  return fmaxf(__uint_as_float(s[0]), __uint_as_float(s[1]));
}
__device__ __forceinline__ float x32_max(float x) {
  auto s = __builtin_amdgcn_permlane32_swap(__float_as_uint(x), __float_as_uint(x), false, false);
  return fmaxf(__uint_as_float(s[0]), __uint_as_float(s[1]));
}
__device__ __forceinline__ float x16_sum(float x) {
  auto s = __builtin_amdgcn_permlane16_swap(__float_as_uint(x), __float_as_uint(x), false, false);
  return __uint_as_float(s[0]) + __uint_as_float(s[1]);
}
__device__ __forceinline__ float x32_sum(float x) {
  auto s = __builtin_amdgcn_permlane32_swap(__float_as_uint(x), __float_as_uint(x), false, false);
  return __uint_as_float(s[0]) + __uint_as_float(s[1]);
}
__device__ __forceinline__ u32 pk_bf16(float lo, float hi) {
  f2 a = {lo, hi};
  return __builtin_bit_cast(u32, __builtin_convertvector(a, bf2));
}
__device__ __forceinline__ v4u tr_frag(LAS unsigned char* p0, LAS unsigned char* p1) {
  const s4 a = __builtin_amdgcn_ds_read_tr16_b64_v4i16((LAS s4*)p0);
  const s4 b = __builtin_amdgcn_ds_read_tr16_b64_v4i16((LAS s4*)p1);
  const v2u x = __builtin_bit_cast(v2u, a), y = __builtin_bit_cast(v2u, b);
  return v4u{x.x, x.y, y.x, y.y};
}
#define BAR() asm volatile("s_waitcnt lgkmcnt(0)\n\ts_barrier" ::: "memory")
#define SCHED() __builtin_amdgcn_sched_barrier(0)
#define STAMP(i) do {} while (0)

__global__ void __launch_bounds__(512, 2) gqa2_decode(Params pa) {
  __shared__ __attribute__((aligned(16))) unsigned char smem[L_END];
  LAS unsigned char* L = (LAS unsigned char*)smem;
  STAMP(0);
  const int bid = blockIdx.x, tid = threadIdx.x, lane = tid & 63;
  const int wave = __builtin_amdgcn_readfirstlane(tid >> 6);
  const int grp = bid >> 4, br = grp >> 3, kvh = grp & 7, team = bid >> 3, tig = team & 1, mem = bid & 7, jg = bid & 15;

  Branch a = pa.b[br];
  asm volatile("" : "+s"(a.x), "+s"(a.wq), "+s"(a.k), "+s"(a.v), "+s"(a.wo), "+s"(a.out), "+s"(a.ws));
  const __amdgpu_buffer_rsrc_t wsr = mk(a.ws, WS_END);
  const u32 epoch_v = __builtin_amdgcn_raw_buffer_load_b32(wsr, WS_EPOCH + br * 256, 0, AUX_SC1);

  v4u xf[2], wqr[4];
  if (wave < 7) {
    const __amdgpu_buffer_rsrc_t xr = mk(a.x, 4 * DM * 2);
    const __amdgpu_buffer_rsrc_t wqs = mk(a.wq, DM * NQ * 2);
    const int sl = 2 * mem + tig;
#pragma unroll
    for (int n = 0; n < 4; ++n)
      wqr[n] = __builtin_amdgcn_raw_buffer_load_b128(wqs, (lane >> 2) * (NQ * 2) + (lane & 3) * 16, (64 * wave + 16 * n) * (NQ * 2) + (kvh * 512 + 32 * sl) * 2, AUX_NT);
#pragma unroll
    for (int ks = 0; ks < 2; ++ks)
      xf[ks] = __builtin_amdgcn_raw_buffer_load_b128(xr, (lane & 15) * (DM * 2) + (lane >> 4) * 16 + ks * 64, wave * 128, 0);
  } else if (mem == 0) {
    if (lane == 0) {
      u32 old = __hip_atomic_exchange((GAS u32*)((unsigned char*)a.ws + WS_CNT + team * 256), 0u, __ATOMIC_RELAXED, __HIP_MEMORY_SCOPE_AGENT);
      asm volatile("" ::"v"(old));
    }
    asm volatile("s_waitcnt vmcnt(0)" ::: "memory");
  }
  SCHED();

  const size_t ubase = (size_t)(2 * tig) * BATCH_BYTES + (size_t)kvh * UNIT_BYTES;
  const __amdgpu_buffer_rsrc_t krs = mk((const unsigned char*)a.k + ubase, BATCH_BYTES + UNIT_BYTES);
  const __amdgpu_buffer_rsrc_t vrs = mk((const unsigned char*)a.v + ubase, BATCH_BYTES + UNIT_BYTES);
  const __amdgpu_buffer_rsrc_t crs = mk((unsigned char*)a.ws + WS_CNT + team * 256, 4);
  const int koff = ((lane >> 2) & 3) * 512 + ((lane >> 4) * 4 + (lane & 3)) * 16;
  const int voff = (lane >> 4) * 512 + (lane & 15) * 16;
  const int toff = lane * 4;
  v4u kf[2][4], vf[2][4];
  u32 tk[2];
  int cur[2], tb[2];
  const int rank = mem * 8 + wave;
#define ISSUE(s_, t_) do { const int so_ = ((t_) >> 10) * BATCH_BYTES + ((t_) & 1023) * 4096; \
    _Pragma("unroll") for (int i_ = 0; i_ < 4; ++i_) kf[s_][i_] = __builtin_amdgcn_raw_buffer_load_b128(krs, koff + (i_ >> 1) * 2048 + (i_ & 1) * 256, so_, AUX_NT); \
    _Pragma("unroll") for (int i_ = 0; i_ < 4; ++i_) vf[s_][i_] = __builtin_amdgcn_raw_buffer_load_b128(vrs, voff + (i_ >> 1) * 2048 + (i_ & 1) * 256, so_, AUX_NT); } while (0)
#define TICKET(s_, live_) do { tk[s_] = (u32)__builtin_amdgcn_raw_ptr_buffer_atomic_add_i32(1, crs, toff, (live_) ? 0 : 4, 0); SCHED(); } while (0)
  cur[0] = rank; cur[1] = rank + 64; tb[0] = rank + 128; tb[1] = rank + 192;
  ISSUE(0, cur[0]);
  TICKET(0, false);
  STAMP(1);

  if (wave < 7) {
    LAS unsigned char* wl = L + L_WQ + wave * 4096;
#pragma unroll
    for (int n = 0; n < 4; ++n) *(LAS v4u*)(wl + n * 1024 + lane * 16) = wqr[n];
    asm volatile("s_waitcnt lgkmcnt(0)" ::: "memory");
    LAS unsigned char* tp = wl + ((lane >> 4) * 8 + ((lane >> 2) & 3)) * 64 + (lane & 3) * 8;
#pragma unroll
    for (int ct = 0; ct < 2; ++ct) {
      f4 d = {0.f, 0.f, 0.f, 0.f};
#pragma unroll
      for (int ks = 0; ks < 2; ++ks) d = mfma(tr_frag(tp + ks * 2048 + ct * 32, tp + ks * 2048 + ct * 32 + 256), xf[ks], d);
      if ((lane & 15) < 4) *(LAS f4*)(L + L_QP + wave * 512 + (lane & 15) * 128 + ct * 64 + (lane >> 4) * 16) = d;
    }
  }
  const u32 tag = (u32)__builtin_amdgcn_readfirstlane((int)epoch_v) + TAG_STEP;
  STAMP(2);
  BAR();
  if (wave == 0) {
    f2 s = {0.f, 0.f};
#pragma unroll
    for (int w = 0; w < 7; ++w) s += *(LAS f2*)(L + L_QP + w * 512 + lane * 8);
    const int sl = 2 * mem + tig;
    __builtin_amdgcn_raw_buffer_store_b64(v2u{pk_bf16(s.x, s.y), tag}, wsr, (lane >> 4) * 2048 + (lane & 15) * 8, WS_QB + grp * 8192 + sl * 128, AUX_SC1);
  }
  SCHED();
  STAMP(3);
  v4u wor[4];
  {
    const __amdgpu_buffer_rsrc_t wos = mk((const unsigned char*)a.wo + (size_t)(kvh * 512 + (jg >> 1) * 64) * (DM * 2), 64 * DM * 2);
#pragma unroll
    for (int n = 0; n < 4; ++n) {
      const int c = tid + 512 * n, row = c / 28, cc = c - row * 28;
      wor[n] = __builtin_amdgcn_raw_buffer_load_b128(wos, row * (DM * 2) + cc * 16, (jg & 1) * 448, AUX_NT);
    }
  }
  SCHED();
  __builtin_amdgcn_s_sleep(QDELAY);
  v4u qv = {0u, 0u, 0u, 0u};
  const int qso = WS_QB + grp * 8192 + (2 * tig + (wave >> 1)) * 2048 + (wave & 1) * 1024;
  if (wave < 4) qv = __builtin_amdgcn_raw_buffer_load_b128(wsr, lane * 16, qso, AUX_SC1);
  SCHED();
  BAR();
  ISSUE(1, cur[1]);
  TICKET(1, false);
  STAMP(4);
#pragma unroll
  for (int n = 0; n < 4; ++n) {
    const int c = tid + 512 * n, row = c / 28, cc = c - row * 28;
    const int kslot = (row & 32) + 4 * (row & 7) + ((row >> 3) & 2) + ((row >> 3) & 1);
    if (c < 1792) *(LAS v4u*)(L + L_WO + kslot * WO_STRIDE + cc * 16) = wor[n];
  }
  if (wave < 4) {
    if (__builtin_amdgcn_ballot_w64(qv.y == tag && qv.w == tag) != ~0ull) {
#pragma unroll 1
      for (u32 spin = 0; spin < SPIN_MAX; ++spin) {
        qv = __builtin_amdgcn_raw_buffer_load_b128(wsr, lane * 16, qso, AUX_SC1);
        if (__builtin_amdgcn_ballot_w64(qv.y == tag && qv.w == tag) == ~0ull) break;
        __builtin_amdgcn_s_sleep(2);
      }
    }
    *(LAS v2u*)(L + L_QF + wave * 512 + lane * 8) = v2u{qv.x, qv.z};
  }
  STAMP(5);
  BAR();

  const bool kg1 = (lane & 4) != 0;
  const int qfo = ((lane >> 3) & 1) * 512 + ((lane >> 4) * 4 + (lane & 3)) * 16;
  v4u qa[2], qb[2];
#define LOADQ(u_) do { _Pragma("unroll") for (int i_ = 0; i_ < 2; ++i_) { const v4u f_ = *(LAS v4u*)(L + L_QF + (u_) * 1024 + qfo + i_ * 256); \
    const v4u z_ = {0u, 0u, 0u, 0u}; qa[i_] = kg1 ? z_ : f_; qb[i_] = kg1 ? f_ : z_; } } while (0)
  LOADQ(0);
  u32 msk[4];
#pragma unroll
  for (int v = 0; v < 4; ++v) msk[v] = (((lane & 7) >> 1) == v) ? ((lane & 1) ? 0xFFFF0000u : 0x0000FFFFu) : 0u;
  constexpr float C2 = 0.0625f * 1.4426950408889634f;
  float m = -INFINITY, l = 0.f;
  f4 acc[2] = {{0.f, 0.f, 0.f, 0.f}, {0.f, 0.f, 0.f, 0.f}};
  int unit = 0;
#define DUMP(u_) do { float ls_ = l; ls_ += dppf<HALF_MIRROR>(ls_); ls_ = x32_sum(x16_sum(ls_)); \
    *(LAS f4*)(L + L_REC + ((u_) * 8 + wave) * 2048 + lane * 16) = acc[0]; \
    *(LAS f4*)(L + L_REC + ((u_) * 8 + wave) * 2048 + 1024 + lane * 16) = acc[1]; \
    if ((lane & 0x37) == 0) *(LAS f2*)(L + L_RML + (u_) * 128 + (lane >> 3) * 64 + wave * 8) = f2{m, ls_}; } while (0)
#define SWITCH() do { DUMP(0); LOADQ(1); m = -INFINITY; l = 0.f; acc[0] = f4{0.f, 0.f, 0.f, 0.f}; acc[1] = f4{0.f, 0.f, 0.f, 0.f}; unit = 1; } while (0)
#pragma unroll 1
  do {
#pragma unroll
    for (int s = 0; s < 2; ++s) {
      if (cur[s] < NT) {
        if (cur[s] >= 1024 && unit == 0) SWITCH();
        f4 d = {0.f, 0.f, 0.f, 0.f};
        d = mfma(kf[s][0], qa[0], d);
        d = mfma(kf[s][1], qa[1], d);
        d = mfma(kf[s][2], qb[0], d);
        d = mfma(kf[s][3], qb[1], d);
        const float t = ((dppf<0x00>(d.x) + dppf<0x55>(d.y)) + (dppf<0xAA>(d.z) + dppf<0xFF>(d.w))) * C2;
        float tm = fmaxf(t, dppf<HALF_MIRROR>(t));
        tm = x32_max(x16_max(tm));
        const float mn = fmaxf(m, __builtin_ceilf(tm));
        const float alpha = __builtin_amdgcn_exp2f(m - mn), p = __builtin_amdgcn_exp2f(t - mn);
        m = mn;
        l = fmaf(l, alpha, p);
        acc[0] *= alpha; acc[1] *= alpha;
        const u32 pk = pk_bf16(p, p), po = dppu<HALF_MIRROR>(pk);
        const v4u ba = {pk & msk[0], pk & msk[1], po & msk[2], po & msk[3]};
        const v4u bb = {po & msk[0], po & msk[1], pk & msk[2], pk & msk[3]};
        acc[0] = mfma(vf[s][0], ba, acc[0]);
        acc[1] = mfma(vf[s][1], ba, acc[1]);
        acc[0] = mfma(vf[s][2], bb, acc[0]);
        acc[1] = mfma(vf[s][3], bb, acc[1]);
      }
      SCHED();
      const int nx = cur[s] < NT ? min(__builtin_amdgcn_readfirstlane((int)tk[s]) + tb[s], NT) : NT;
      cur[s] = nx; tb[s] = NSTATIC;
      ISSUE(s, nx);
      TICKET(s, nx < NT);
    }
  } while (min(cur[0], cur[1]) < NT);
  STAMP(6);
  if (unit == 0) SWITCH();
  DUMP(1);
  v4u wa[2][2];
  {
    const int wv = wave < 7 ? wave : 0;
    LAS unsigned char* tp = L + L_WO + ((lane >> 4) * 8 + ((lane >> 2) & 3)) * WO_STRIDE + (lane & 3) * 8 + wv * 64;
#pragma unroll
    for (int ct = 0; ct < 2; ++ct)
#pragma unroll
      for (int ks = 0; ks < 2; ++ks) wa[ct][ks] = tr_frag(tp + ks * 32 * WO_STRIDE + ct * 32, tp + (ks * 32 + 4) * WO_STRIDE + ct * 32);
  }
  BAR();
  STAMP(7);

  {
    const int u = tid >> 8, i = (tid >> 7) & 1, ln = (tid >> 1) & 63, rp = tid & 1, g = (ln >> 3) & 1;
    f2 ml[8];
#pragma unroll
    for (int w = 0; w < 8; w += 2) {
      const f4 t4 = *(LAS f4*)(L + L_RML + u * 128 + g * 64 + w * 8);
      ml[w] = f2{t4.x, t4.y}; ml[w + 1] = f2{t4.z, t4.w};
    }
    float M = ml[0].x;
#pragma unroll
    for (int w = 1; w < 8; ++w) M = fmaxf(M, ml[w].x);
    const float Ms = (M == -INFINITY) ? 0.f : M;
    float Ls = 0.f; f2 o = {0.f, 0.f};
#pragma unroll
    for (int w = 0; w < 8; ++w) {
      const float wt = __builtin_amdgcn_exp2f(ml[w].x - Ms);
      const f2 r = *(LAS f2*)(L + L_REC + (u * 8 + w) * 2048 + i * 1024 + ln * 16 + rp * 8);
      Ls = fmaf(wt, ml[w].y, Ls); o.x = fmaf(wt, r.x, o.x); o.y = fmaf(wt, r.y, o.y);
    }
    const int bp = (grp * 4 + 2 * tig + u) * 8 + mem;
    const int slice = g * 4 + i * 2 + (ln >> 5), e = ((ln >> 4) & 1) * 16 + (ln & 7) * 2 + rp;
    __builtin_amdgcn_raw_buffer_store_b128(v4u{__float_as_uint(o.x), tag, __float_as_uint(o.y), tag}, wsr, bp * 4096 + slice * 512 + e * 16, WS_P1, AUX_SC1);
    if ((tid & 0xEF) == 0)
      __builtin_amdgcn_raw_buffer_store_b128(v4u{__float_as_uint(M), tag, __float_as_uint(Ls), tag}, wsr, bp * 32 + g * 16, WS_ML, AUX_SC1);
  }
  STAMP(8);
  {
    const int slice = jg >> 1, b = tid >> 7, p = (tid >> 2) & 7, e = (tid & 3) + ((tid >> 5) & 3) * 4;
    const int bp = (grp * 4 + b) * 8 + p;
    const int po = bp * 4096 + slice * 512 + e * 16, mo = bp * 32 + (slice >> 2) * 16;
    v4u r0, r1, rm;
#pragma unroll 1
    for (u32 spin = 0;; ++spin) {
      r0 = __builtin_amdgcn_raw_buffer_load_b128(wsr, po, WS_P1, AUX_SC1);
      r1 = __builtin_amdgcn_raw_buffer_load_b128(wsr, po + 256, WS_P1, AUX_SC1);
      rm = __builtin_amdgcn_raw_buffer_load_b128(wsr, mo, WS_ML, AUX_SC1);
      const bool ok = r0.y == tag && r0.w == tag && r1.y == tag && r1.w == tag && rm.y == tag && rm.w == tag;
      if (__builtin_amdgcn_ballot_w64(ok) == ~0ull || spin >= SPIN_MAX) break;
      __builtin_amdgcn_s_sleep(1);
    }
    const float mp = __uint_as_float(rm.x);
    float M = fmaxf(mp, dppf<ROR4>(mp)); M = fmaxf(M, dppf<ROR8>(M)); M = x16_max(M);
    const float wt = __builtin_amdgcn_exp2f(mp - M);
    float v[5] = {wt * __uint_as_float(rm.z), wt * __uint_as_float(r0.x), wt * __uint_as_float(r0.z), wt * __uint_as_float(r1.x), wt * __uint_as_float(r1.z)};
#pragma unroll
    for (int q = 0; q < 5; ++q) { v[q] += dppf<ROR4>(v[q]); v[q] += dppf<ROR8>(v[q]); v[q] = x16_sum(v[q]); }
    const float inv = 1.0f / v[0];
    if (p == 0) {
      *(LAS u32*)(L + L_X + b * 128 + e * 4) = pk_bf16(v[1] * inv, v[2] * inv);
      *(LAS u32*)(L + L_X + b * 128 + 64 + e * 4) = pk_bf16(v[3] * inv, v[4] * inv);
    }
  }
  STAMP(9);
  BAR();
  if (wave < 7) {
    v4u xb[2];
#pragma unroll
    for (int ks = 0; ks < 2; ++ks) xb[ks] = *(LAS v4u*)(L + L_X + (lane & 3) * 128 + ks * 64 + (lane >> 4) * 16);
    const int so = WS_P2 + ((br * 2 + (jg & 1)) * 64 + kvh * 8 + (jg >> 1)) * 7168 + wave * 256;
#pragma unroll
    for (int ct = 0; ct < 2; ++ct) {
      f4 d = {0.f, 0.f, 0.f, 0.f};
      d = mfma(wa[ct][0], xb[0], d);
      d = mfma(wa[ct][1], xb[1], d);
      const bool hi = (lane & 4) != 0;
      const float y0 = hi ? d.z : d.x, y1 = hi ? d.w : d.y;
      if ((lane & 8) == 0)
        __builtin_amdgcn_raw_buffer_store_b128(v4u{__float_as_uint(y0), tag, __float_as_uint(y1), tag}, wsr,
                                               (lane & 3) * 1792 + ct * 128 + (lane >> 4) * 32 + ((lane >> 2) & 1) * 16, so, AUX_SC1);
    }
  } else {
    const int ci = bid & 127;
    if (ci < 112) {
      const int ch = ci / 56, b = (ci - ch * 56) / 14, ln = ci - ch * 56 - b * 14;
      const int so = WS_P2 + (br * 2 + ch) * 64 * 7168 + b * 1792 + ln * 128;
      const int vo = (lane >> 3) * 7168 + (lane & 7) * 16;
      v4u r[8];
#pragma unroll 1
      for (u32 spin = 0;; ++spin) {
        bool ok = true;
#pragma unroll
        for (int n = 0; n < 8; ++n) r[n] = __builtin_amdgcn_raw_buffer_load_b128(wsr, vo + n * 8 * 7168, so, AUX_SC1);
#pragma unroll
        for (int n = 0; n < 8; ++n) ok = ok && r[n].y == tag && r[n].w == tag;
        if (__builtin_amdgcn_ballot_w64(ok) == ~0ull || spin >= SPIN_MAX) break;
        __builtin_amdgcn_s_sleep(1);
      }
      float y0 = 0.f, y1 = 0.f;
#pragma unroll
      for (int n = 0; n < 8; ++n) { y0 += __uint_as_float(r[n].x); y1 += __uint_as_float(r[n].z); }
      y0 += dppf<ROR8>(y0); y1 += dppf<ROR8>(y1);
      y0 = x32_sum(x16_sum(y0)); y1 = x32_sum(x16_sum(y1));
      if (lane < 8) ((GAS u32*)a.out)[(b * 896 + br * 448 + ch * 224 + ln * 16) / 2 + lane] = pk_bf16(y0, y1);
      if (ci == 0 && lane == 0) __builtin_amdgcn_raw_buffer_store_b32(tag, wsr, WS_EPOCH + br * 256, 0, AUX_SC1);
    }
  }
}

extern "C" void kernel_launch(void* const* d_in, const int* in_sizes, int n_in, void* d_out, int out_size, void* d_ws, size_t ws_size,
                              hipStream_t stream) {
  Params p;
  for (int i = 0; i < 2; ++i) {
    p.b[i].x = d_in[5 * i + 0]; p.b[i].k = d_in[5 * i + 1]; p.b[i].v = d_in[5 * i + 2]; p.b[i].wq = d_in[5 * i + 3]; p.b[i].wo = d_in[5 * i + 4];
    p.b[i].out = d_out; p.b[i].ws = d_ws; p.b[i].ws2 = d_ws;
  }
  hipLaunchKernelGGL(gqa2_decode, dim3(256), dim3(512), 0, stream, p);
}
```

```cpp
#include <hip/hip_runtime.h>
#include <hip/hip_bf16.h>

typedef unsigned u32;
typedef unsigned long long u64;
typedef u32 v4u __attribute__((ext_vector_type(4)));
typedef u32 v2u __attribute__((ext_vector_type(2)));
typedef float f4 __attribute__((ext_vector_type(4)));
typedef float f2 __attribute__((ext_vector_type(2)));
typedef short s4 __attribute__((ext_vector_type(4)));
typedef __bf16 bf8 __attribute__((ext_vector_type(8)));
typedef __bf16 bf2 __attribute__((ext_vector_type(2)));
#define LAS __attribute__((address_space(3)))
#define GAS __attribute__((address_space(1)))

#define DIAG 0
#define SDELAY 8
#define QDELAY 14
#define RSTATIC 50
#define DRYRUN 1

constexpr int DM = 448, NQ = 4096, SK = 8192;
constexpr int UNIT_BYTES = SK * 512;
constexpr int NT = 1024;
constexpr int SBLK = 4 * RSTATIC;
constexpr int NSTATIC = 4 * SBLK;
constexpr int AUX_NT = 2, AUX_SC1 = 16;
constexpr int RSRC3 = 0x00020000;
constexpr u32 TAG_STEP = 0x9E3779B9u;
constexpr u32 SPIN_MAX = 1u << 18;

constexpr int WS_EPOCH = 0;
constexpr int WS_CNT = 4096;
constexpr int WS_ML = WS_CNT + 64 * 256;
constexpr int WS_QB = WS_ML + 16 * 2048;
constexpr int WS_P1 = WS_QB + 16 * 8192;
constexpr int WS_P2 = WS_P1 + 16 * 32768;
constexpr int WS_END = WS_P2 + 2 * 2 * 64 * 7168;
constexpr int WS_DIAG = 8 << 20;

constexpr int WO_STRIDE = 464;
constexpr int L_WQ = 0;
constexpr int L_QP = 32768;
constexpr int L_QC = L_QP + 2048;
constexpr int L_QF = L_QC + 16;
constexpr int L_WO = L_QF + 1024;
constexpr int L_REC = L_WO + 64 * WO_STRIDE;
constexpr int L_RML = L_REC + 8192;
constexpr int L_X = L_RML + 64;
constexpr int L_END = 82 * 1024;
static_assert(L_X + 512 <= L_END, "LDS map");
static_assert((L_WO % 16) == 0 && (L_REC % 16) == 0, "LDS alignment");

struct Branch { const void* x; const void* wq; const void* k; const void* v; const void* wo; void* out; void* ws; void* ws2; };
struct Params { Branch b[2]; };

__device__ __forceinline__ __amdgpu_buffer_rsrc_t mk(const void* p, int bytes) {
  return __builtin_amdgcn_make_buffer_rsrc((void*)p, 0, bytes, RSRC3);
}
__device__ __forceinline__ void st16(__amdgpu_buffer_rsrc_t r, int off, u32 v0, u32 v1, u32 tag) {
  __builtin_amdgcn_raw_buffer_store_b128(v4u{v0, tag, v1, tag}, r, off, 0, AUX_SC1);
}
__device__ __forceinline__ v4u ld16(__amdgpu_buffer_rsrc_t r, int off) { return __builtin_amdgcn_raw_buffer_load_b128(r, off, 0, AUX_SC1); }
__device__ __forceinline__ f4 mfma(v4u a, v4u b, f4 c) {
  return __builtin_amdgcn_mfma_f32_16x16x32_bf16(__builtin_bit_cast(bf8, a), __builtin_bit_cast(bf8, b), c, 0, 0, 0);
}
template <int CTRL> __device__ __forceinline__ float dppf(float x) {
  return __builtin_bit_cast(float, __builtin_amdgcn_mov_dpp(__builtin_bit_cast(int, x), CTRL, 0xf, 0xf, true));
}
template <int CTRL> __device__ __forceinline__ u32 dppu(u32 x) {
  return (u32)__builtin_amdgcn_mov_dpp((int)x, CTRL, 0xf, 0xf, true);
}
constexpr int HALF_MIRROR = 0x141, ROR8 = 0x128;
__device__ __forceinline__ float x16_max(float x) {
  auto s = __builtin_amdgcn_permlane16_swap(__float_as_uint(x), __float_as_uint(x), false, false);
  return fmaxf(__uint_as_float(s[0]), __uint_as_float(s[1]));
}
__device__ __forceinline__ float x32_max(float x) {
  auto s = __builtin_amdgcn_permlane32_swap(__float_as_uint(x), __float_as_uint(x), false, false);
  return fmaxf(__uint_as_float(s[0]), __uint_as_float(s[1]));
}
__device__ __forceinline__ float x16_sum(float x) {
  auto s = __builtin_amdgcn_permlane16_swap(__float_as_uint(x), __float_as_uint(x), false, false);
  return __uint_as_float(s[0]) + __uint_as_float(s[1]);
}
__device__ __forceinline__ float x32_sum(float x) {
  auto s = __builtin_amdgcn_permlane32_swap(__float_as_uint(x), __float_as_uint(x), false, false);
  return __uint_as_float(s[0]) + __uint_as_float(s[1]);
}
__device__ __forceinline__ u32 pk_bf16(float lo, float hi) {
  f2 a = {lo, hi};
  return __builtin_bit_cast(u32, __builtin_convertvector(a, bf2));
}
__device__ __forceinline__ float bf_lo(u32 x) { return __uint_as_float(x << 16); }
__device__ __forceinline__ float bf_hi(u32 x) { return __uint_as_float(x & 0xFFFF0000u); }
__device__ __forceinline__ v4u tr_frag(LAS unsigned char* p0, LAS unsigned char* p1) {
  const s4 a = __builtin_amdgcn_ds_read_tr16_b64_v4i16((LAS s4*)p0);
  const s4 b = __builtin_amdgcn_ds_read_tr16_b64_v4i16((LAS s4*)p1);
  const v2u x = __builtin_bit_cast(v2u, a), y = __builtin_bit_cast(v2u, b);
  return v4u{x.x, x.y, y.x, y.y};
}
__device__ __forceinline__ bool all_lanes(bool ok) { return __builtin_amdgcn_ballot_w64(ok) == ~0ull; }
#define BAR() asm volatile("s_waitcnt lgkmcnt(0)\n\ts_barrier" ::: "memory")
#define LDSWAIT() asm volatile("s_waitcnt lgkmcnt(0)" ::: "memory")
#define SCHED() __builtin_amdgcn_sched_barrier(0)
#define FRESH() asm volatile("" ::: "memory")
#define STAMP(i) do {} while (0)

__global__ void __launch_bounds__(512, 2) gqa2_decode(Params pa) {
  __shared__ __attribute__((aligned(16))) unsigned char smem[L_END];
  LAS unsigned char* L = (LAS unsigned char*)smem;
  STAMP(0);
  const int bid = blockIdx.x, tid = threadIdx.x, lane = tid & 63;
  const int wave = __builtin_amdgcn_readfirstlane(tid >> 6);
  const int grp = bid >> 4, br = grp >> 3, kvh = grp & 7, jg = bid & 15, mem = bid & 7;
  const int bat = 2 * (jg >> 3) + (((mem + 1) >> 1) & 1), pos = mem >> 1;
  const bool helper = wave < 4;
  Branch a = pa.b[br];
  if (tid == 0) *(LAS v4u*)(L + L_QC) = v4u{0u, 0u, 0u, 0u};
  asm volatile("s_waitcnt lgkmcnt(0)\n\ts_barrier" : "+s"(a.x), "+s"(a.wq), "+s"(a.k), "+s"(a.v), "+s"(a.wo), "+s"(a.out), "+s"(a.ws)::"memory");
  const __amdgpu_buffer_rsrc_t wsr = mk(a.ws, WS_END);

  unsigned char* cntp = (unsigned char*)a.ws + WS_CNT + (grp * 4 + bat) * 256;
  u32 tag;
  v4u wa[2][2];
  LAS unsigned char* wtp = L + L_WO + ((lane >> 4) * 8 + ((lane >> 2) & 3)) * WO_STRIDE + (lane & 3) * 8 + (wave < 7 ? wave : 0) * 64;
#define LOADW() do { _Pragma("unroll") for (int ct = 0; ct < 2; ++ct) _Pragma("unroll") for (int ks = 0; ks < 2; ++ks) \
    wa[ct][ks] = tr_frag(wtp + ks * 32 * WO_STRIDE + ct * 32, wtp + (ks * 32 + 4) * WO_STRIDE + ct * 32); } while (0)

  if (helper) {
    __builtin_amdgcn_s_setprio(3);
    const u32 epoch_v = __builtin_amdgcn_raw_buffer_load_b32(wsr, WS_EPOCH + br * 256, 0, AUX_SC1);
    const int sl = 2 * mem + (jg >> 3);
    u32 cinit = 0;
    if (wave == 3 && pos == 0 && lane == 0) cinit = __hip_atomic_exchange((GAS u32*)cntp, 0u, __ATOMIC_RELAXED, __HIP_MEMORY_SCOPE_AGENT);
    const __amdgpu_buffer_rsrc_t xr = mk(a.x, 4 * DM * 2);
    const __amdgpu_buffer_rsrc_t wqs = mk(a.wq, DM * NQ * 2);
    v4u xf[4], wr[8];
#pragma unroll
    for (int n = 0; n < 8; ++n)
      wr[n] = __builtin_amdgcn_raw_buffer_load_b128(wqs, (lane >> 2) * (NQ * 2) + (lane & 3) * 16, (32 * wave + 128 * (n >> 1) + 16 * (n & 1)) * (NQ * 2) + (kvh * 512 + 32 * sl) * 2, AUX_NT);
#pragma unroll
    for (int ks = 0; ks < 4; ++ks)
      xf[ks] = __builtin_amdgcn_raw_buffer_load_b128(xr, (lane & 15) * (DM * 2) + (lane >> 4) * 16 + ks * 256, wave * 64, 0);
    SCHED();
    STAMP(1);
    LAS unsigned char* wl = L + L_WQ + wave * 8192;
#pragma unroll
    for (int n = 0; n < 8; ++n) *(LAS v4u*)(wl + n * 1024 + lane * 16) = wr[n];
    LDSWAIT();
    LAS unsigned char* tp = wl + ((lane >> 4) * 8 + ((lane >> 2) & 3)) * 64 + (lane & 3) * 8;
    f4 d0 = {0.f, 0.f, 0.f, 0.f}, d1 = {0.f, 0.f, 0.f, 0.f};
#pragma unroll
    for (int ks = 0; ks < 4; ++ks) {
      d0 = mfma(tr_frag(tp + ks * 2048, tp + ks * 2048 + 256), xf[ks], d0);
      d1 = mfma(tr_frag(tp + ks * 2048 + 32, tp + ks * 2048 + 32 + 256), xf[ks], d1);
    }
    asm volatile("" ::"v"(cinit));
    tag = (u32)__builtin_amdgcn_readfirstlane((int)epoch_v) + TAG_STEP;
    if ((lane & 15) < 4) {
      *(LAS f4*)(L + L_QP + wave * 512 + (lane & 15) * 128 + (lane >> 4) * 16) = d0;
      *(LAS f4*)(L + L_QP + wave * 512 + (lane & 15) * 128 + 64 + (lane >> 4) * 16) = d1;
    }
    LDSWAIT();
    if (wave != 0) {
      *(volatile LAS u32*)(L + L_QC + wave * 4) = 1u;
    } else {
#pragma unroll 1
      for (u32 spin = 0; spin < (SPIN_MAX << 4); ++spin) {
        const v4u f = *(volatile LAS v4u*)(L + L_QC);
        if (__builtin_amdgcn_readfirstlane((int)(f.y & f.z & f.w))) break;
      }
      FRESH();
      f2 s = *(LAS f2*)(L + L_QP + lane * 8);
#pragma unroll
      for (int w = 1; w < 4; ++w) s += *(LAS f2*)(L + L_QP + w * 512 + lane * 8);
      __builtin_amdgcn_raw_buffer_store_b64(v2u{pk_bf16(s.x, s.y), tag}, wsr, WS_QB + grp * 8192 + sl * 128 + (lane >> 4) * 2048 + (lane & 15) * 8, 0, AUX_SC1);
    }
    SCHED();
    STAMP(2);
    const int qoff = WS_QB + grp * 8192 + bat * 2048 + tid * 8;
#define QPOLL(dst_) do { __builtin_amdgcn_s_sleep(QDELAY); FRESH(); dst_ = __builtin_amdgcn_raw_buffer_load_b64(wsr, qoff, 0, AUX_SC1); SCHED(); } while (0)
    v2u q0, q1, q2, q3;
    QPOLL(q0); QPOLL(q1); QPOLL(q2); QPOLL(q3);
    STAMP(3);
#define QCNT(n_)
    if (!all_lanes(q0.y == tag)) {
      q0 = q1; QCNT(1);
      if (!all_lanes(q0.y == tag)) {
        q0 = q2; QCNT(1);
        if (!all_lanes(q0.y == tag)) {
          q0 = q3; QCNT(1);
          if (!all_lanes(q0.y == tag)) {
            u32 spin = 0;
#pragma unroll 1
            do { QPOLL(q0); QCNT(100); } while (!all_lanes(q0.y == tag) && ++spin < SPIN_MAX);
          }
        }
      }
    }
    *(LAS u32*)(L + L_QF + tid * 4) = q0.x;
    __builtin_amdgcn_s_setprio(0);
    STAMP(4); STAMP(5);
    BAR();
    asm volatile("" ::"v"(q1), "v"(q2), "v"(q3));
    LOADW();
    STAMP(6);
  } else {
    const size_t ubase = (size_t)(bat * 8 + kvh) * UNIT_BYTES;
    const __amdgpu_buffer_rsrc_t krs = mk((const unsigned char*)a.k + ubase, UNIT_BYTES);
    const __amdgpu_buffer_rsrc_t vrs = mk((const unsigned char*)a.v + ubase, UNIT_BYTES);
    const __amdgpu_buffer_rsrc_t crs = mk(cntp, 4);
    const int koff = ((lane >> 2) & 3) * 512 + ((lane >> 4) * 4 + (lane & 3)) * 16;
    const int voff = (lane >> 4) * 512 + (lane & 15) * 16;
    const int toff = lane * 4;
    v4u kf[4][4], vf[4][4];
    u32 tk[2];
    int tb[2];
    const int slim = (pos + 1) * SBLK;
    int snext = pos * SBLK + wave - 4;
#define ISSUE(s_, t_) do { const int so_ = (t_) * 4096; \
    _Pragma("unroll") for (int i_ = 0; i_ < 4; ++i_) kf[s_][i_] = __builtin_amdgcn_raw_buffer_load_b128(krs, koff + (i_ >> 1) * 2048 + (i_ & 1) * 256, so_, AUX_NT); \
    _Pragma("unroll") for (int i_ = 0; i_ < 4; ++i_) vf[s_][i_] = __builtin_amdgcn_raw_buffer_load_b128(vrs, voff + (i_ >> 1) * 2048 + (i_ & 1) * 256, so_, AUX_NT); SCHED(); } while (0)
#define TICKET(s_, live_) do { const bool st_ = snext < slim; tb[s_] = st_ ? snext : NSTATIC; snext += 4; \
    tk[s_] = (u32)__builtin_amdgcn_raw_ptr_buffer_atomic_add_i32(1, crs, toff, (st_ || !(live_)) ? 4 : 0, 0); SCHED(); } while (0)
#define NEXT(dst_, s_) do { asm volatile("" : "+v"(tk[s_])::"memory"); dst_ = __builtin_amdgcn_readfirstlane((int)tk[s_]) + tb[s_]; } while (0)
    __builtin_amdgcn_s_sleep(SDELAY);
    ISSUE(1, snext);
    STAMP(1);
    const u32 epoch_v = __builtin_amdgcn_raw_buffer_load_b32(wsr, WS_EPOCH + br * 256, 0, AUX_SC1);
    const __amdgpu_buffer_rsrc_t wos = mk((const unsigned char*)a.wo + (size_t)(kvh * 512 + (jg >> 1) * 64) * (DM * 2), 64 * DM * 2);
    v4u wr[7];
#pragma unroll
    for (int n = 0; n < 7; ++n) {
      const int c = tid - 256 + 256 * n, row = c / 28, cc = c - row * 28;
      wr[n] = __builtin_amdgcn_raw_buffer_load_b128(wos, row * (DM * 2) + cc * 16, (jg & 1) * 448, AUX_NT);
    }
    SCHED();
    ISSUE(2, snext + 4);
    ISSUE(3, snext + 8);
    ISSUE(0, snext + 12);
    snext += 24;
    STAMP(4);
    u32 ev = epoch_v;
    asm volatile("" : "+v"(ev));
    tag = (u32)__builtin_amdgcn_readfirstlane((int)ev) + TAG_STEP;
#pragma unroll
    for (int n = 0; n < 7; ++n) {
      const int c = tid - 256 + 256 * n, row = c / 28, cc = c - row * 28;
      const int kslot = (row & 32) + 4 * (row & 7) + ((row >> 3) & 3);
      *(LAS v4u*)(L + L_WO + kslot * WO_STRIDE + cc * 16) = wr[n];
    }
    u32 msk[4];
#pragma unroll
    for (int v = 0; v < 4; ++v) msk[v] = (((lane & 7) >> 1) == v) ? ((lane & 1) ? 0xFFFF0000u : 0x0000FFFFu) : 0u;
    STAMP(5);
    BAR();
    v4u qa[2], qb[2];
#pragma unroll
    for (int i = 0; i < 2; ++i) {
      const v4u f = *(LAS v4u*)(L + L_QF + ((lane >> 3) & 1) * 512 + ((lane >> 4) * 4 + (lane & 3)) * 16 + i * 256);
      const v4u z = {0u, 0u, 0u, 0u};
      qa[i] = (lane & 4) ? z : f; qb[i] = (lane & 4) ? f : z;
    }
    constexpr float C2 = 0.0625f * 1.4426950408889634f;
    float m = -INFINITY, l = 0.f;
    f4 acc[2] = {{0.f, 0.f, 0.f, 0.f}, {0.f, 0.f, 0.f, 0.f}};
#define COUNT()
#define TILE(s_) do { \
      asm volatile("" : "+v"(kf[s_][0])::"memory"); \
      f4 d_ = {0.f, 0.f, 0.f, 0.f}; \
      d_ = mfma(kf[s_][0], qa[0], d_); d_ = mfma(kf[s_][1], qa[1], d_); d_ = mfma(kf[s_][2], qb[0], d_); d_ = mfma(kf[s_][3], qb[1], d_); \
      const float t_ = ((dppf<0x00>(d_.x) + dppf<0x55>(d_.y)) + (dppf<0xAA>(d_.z) + dppf<0xFF>(d_.w))) * C2; \
      const float tm_ = x32_max(x16_max(fmaxf(t_, dppf<HALF_MIRROR>(t_)))); \
      const float mn_ = fmaxf(m, __builtin_ceilf(tm_)); \
      const float al_ = __builtin_amdgcn_exp2f(m - mn_), p_ = __builtin_amdgcn_exp2f(t_ - mn_); \
      m = mn_; l = fmaf(l, al_, p_); acc[0] *= al_; acc[1] *= al_; \
      const u32 pk_ = pk_bf16(p_, p_), po_ = dppu<HALF_MIRROR>(pk_); \
      const v4u ba_ = {pk_ & msk[0], pk_ & msk[1], po_ & msk[2], po_ & msk[3]}; \
      const v4u bb_ = {po_ & msk[0], po_ & msk[1], pk_ & msk[2], pk_ & msk[3]}; \
      acc[0] = mfma(vf[s_][0], ba_, acc[0]); acc[1] = mfma(vf[s_][1], ba_, acc[1]); \
      acc[0] = mfma(vf[s_][2], bb_, acc[0]); acc[1] = mfma(vf[s_][3], bb_, acc[1]); \
      asm volatile("" : "+v"(acc[0]), "+v"(acc[1]), "+v"(l), "+v"(m)::"memory"); \
      COUNT(); SCHED(); } while (0)
    TILE(1);
    ISSUE(1, snext - 8);
    TICKET(1, false);
    TILE(2);
    TILE(3);
    TILE(0);
    int n0 = snext - 8, n1;
#pragma unroll 1
    do {
      ISSUE(0, n0); TICKET(0, true);
      TILE(1);
      NEXT(n1, 1);
      n1 = min(n1, NT);
      ISSUE(1, n1); TICKET(1, n1 < NT);
      TILE(0);
      NEXT(n0, 0);
    } while (n0 < NT);
    if (n1 < NT) TILE(1);
    STAMP(6);
    float ls = l; ls += dppf<HALF_MIRROR>(ls); ls = x32_sum(x16_sum(ls));
    *(LAS f4*)(L + L_REC + (wave - 4) * 2048 + lane * 16) = acc[0];
    *(LAS f4*)(L + L_REC + (wave - 4) * 2048 + 1024 + lane * 16) = acc[1];
    if ((lane & 0x37) == 0) *(LAS f2*)(L + L_RML + (lane >> 3) * 32 + (wave - 4) * 8) = f2{m, ls};
  }

  int pass = DRYRUN ? (wave >> 2) : 1;
#pragma unroll 1
  for (; pass < 2; ++pass) {
    asm volatile("" : "+s"(pass));
    const bool dry = pass == 0;
    const __amdgpu_buffer_rsrc_t hs = mk(a.ws, dry ? 0 : WS_END);
    if (!dry) BAR();
    STAMP(7);
    if (helper) {
      if (wave < 2) {
        const int g = (lane >> 3) & 1;
        const f4 ml0 = *(LAS f4*)(L + L_RML + g * 32), ml1 = *(LAS f4*)(L + L_RML + g * 32 + 16);
        f4 rc[4];
#pragma unroll
        for (int w = 0; w < 4; ++w) rc[w] = *(LAS f4*)(L + L_REC + w * 2048 + wave * 1024 + lane * 16);
        const float M = fmaxf(fmaxf(ml0.x, ml0.z), fmaxf(ml1.x, ml1.z));
        const float w0 = __builtin_amdgcn_exp2f(ml0.x - M), w1 = __builtin_amdgcn_exp2f(ml0.z - M), w2 = __builtin_amdgcn_exp2f(ml1.x - M), w3 = __builtin_amdgcn_exp2f(ml1.z - M);
        const f4 o = (w0 * rc[0] + w1 * rc[1]) + (w2 * rc[2] + w3 * rc[3]);
        const int slice = g * 4 + wave * 2 + (lane >> 5), e = ((lane >> 4) & 1) * 8 + (lane & 7);
        st16(hs, WS_P1 + grp * 32768 + slice * 4096 + (bat * 4 + pos) * 256 + e * 16, pk_bf16(o.x, o.y), pk_bf16(o.z, o.w), tag);
        const float Ls = (w0 * ml0.y + w1 * ml0.w) + (w2 * ml1.y + w3 * ml1.w);
        if (tid >= 4 && tid < 12) st16(hs, WS_ML + grp * 2048 + (bat * 4 + pos) * 128 + (tid - 4) * 16, __float_as_uint(M), __float_as_uint(Ls), tag);
      }
      STAMP(8);
      const int slice = jg >> 1;
      const int po = WS_P1 + grp * 32768 + slice * 4096 + tid * 16, mo = WS_ML + grp * 2048 + (tid >> 4) * 128 + (slice >> 2) * 64;
      v4u r, rm;
#pragma unroll 1
      for (u32 spin = 0;; ++spin) {
        FRESH();
        r = ld16(hs, po);
        rm = ld16(hs, mo);
        if (all_lanes((r.y == tag) & (r.w == tag) & (rm.y == tag) & (rm.w == tag)) || spin >= SPIN_MAX || dry) break;
      }
      const float mp = __uint_as_float(rm.x);
      const float wt = __builtin_amdgcn_exp2f(mp - x32_max(x16_max(mp)));
      float v[5] = {wt * __uint_as_float(rm.z), wt * bf_lo(r.x), wt * bf_hi(r.x), wt * bf_lo(r.z), wt * bf_hi(r.z)};
#pragma unroll
      for (int q = 0; q < 5; ++q) v[q] = x32_sum(x16_sum(v[q]));
      const float inv = 1.0f / v[0];
      if (lane < 16) *(LAS v2u*)(L + L_X + wave * 128 + lane * 8) = v2u{pk_bf16(v[1] * inv, v[2] * inv), pk_bf16(v[3] * inv, v[4] * inv)};
    } else {
      LOADW();
      STAMP(8);
    }
    STAMP(9);
    if (!dry) BAR();
    if (wave < 7) {
      v4u xb[2];
#pragma unroll
      for (int ks = 0; ks < 2; ++ks) xb[ks] = *(LAS v4u*)(L + L_X + (lane & 3) * 128 + ks * 64 + (lane >> 4) * 16);
      const int so = WS_P2 + ((br * 2 + (jg & 1)) * 64 + kvh * 8 + (jg >> 1)) * 7168 + wave * 1024;
      f4 d0 = {0.f, 0.f, 0.f, 0.f}, d1 = {0.f, 0.f, 0.f, 0.f};
      d0 = mfma(wa[0][0], xb[0], d0);
      d1 = mfma(wa[1][0], xb[0], d1);
      d0 = mfma(wa[0][1], xb[1], d0);
      d1 = mfma(wa[1][1], xb[1], d1);
      const f4 d = (lane & 8) ? d1 : d0;
      const bool hi = (lane & 4) != 0;
      st16(hs, so + ((lane >> 3) & 1) * 512 + (lane >> 4) * 128 + (lane & 7) * 16, __float_as_uint(hi ? d.z : d.x), __float_as_uint(hi ? d.w : d.y), tag);
    }
    const int ci = bid & 127;
    if ((wave == 7 || dry) && ci < 112) {
      const int ch = ci / 56, ln = ci - ch * 56;
      const int vo = WS_P2 + (br * 2 + ch) * 64 * 7168 + ln * 128 + (lane >> 3) * 7168 + (lane & 7) * 16;
      v4u r[8];
#pragma unroll 1
      for (u32 spin = 0;; ++spin) {
        bool ok = true;
        FRESH();
#pragma unroll
        for (int n = 0; n < 8; ++n) r[n] = ld16(hs, vo + n * 8 * 7168);
#pragma unroll
        for (int n = 0; n < 8; ++n) ok = ok & (r[n].y == tag) & (r[n].w == tag);
        if (all_lanes(ok) || spin >= SPIN_MAX || dry) break;
      }
      float y0 = 0.f, y1 = 0.f;
#pragma unroll
      for (int n = 0; n < 8; ++n) { y0 += __uint_as_float(r[n].x); y1 += __uint_as_float(r[n].z); }
      y0 += dppf<ROR8>(y0); y1 += dppf<ROR8>(y1);
      y0 = x32_sum(x16_sum(y0)); y1 = x32_sum(x16_sum(y1));
      const __amdgpu_buffer_rsrc_t os = mk(a.out, dry ? 0 : 4 * 896 * 2);
      __builtin_amdgcn_raw_buffer_store_b32(pk_bf16(y0, y1), os, lane < 8 ? ((lane & 3) * 896 + br * 448 + ch * 224 + ln * 4) * 2 + (lane >> 2) * 4 : 0x40000000, 0, 0);
      if (ci == 0) __builtin_amdgcn_raw_buffer_store_b32(tag, hs, lane == 0 ? WS_EPOCH + br * 256 : 0x40000000, 0, AUX_SC1);
    }
  }
}

extern "C" void kernel_launch(void* const* d_in, const int* in_sizes, int n_in, void* d_out, int out_size, void* d_ws, size_t ws_size,
                              hipStream_t stream) {
  Params p;
  for (int i = 0; i < 2; ++i) {
    p.b[i].x = d_in[5 * i + 0]; p.b[i].k = d_in[5 * i + 1]; p.b[i].v = d_in[5 * i + 2]; p.b[i].wq = d_in[5 * i + 3]; p.b[i].wo = d_in[5 * i + 4];
    p.b[i].out = d_out; p.b[i].ws = d_ws; p.b[i].ws2 = d_ws;
  }
  hipLaunchKernelGGL(gqa2_decode, dim3(256), dim3(512), 0, stream, p);
}
```

```cpp
#include <hip/hip_runtime.h>
#include <hip/hip_bf16.h>

typedef unsigned u32;
typedef unsigned long long u64;
typedef u32 v4u __attribute__((ext_vector_type(4)));
typedef u32 v2u __attribute__((ext_vector_type(2)));
typedef float f4 __attribute__((ext_vector_type(4)));
typedef float f2 __attribute__((ext_vector_type(2)));
typedef short s4 __attribute__((ext_vector_type(4)));
typedef __bf16 bf8 __attribute__((ext_vector_type(8)));
typedef __bf16 bf2 __attribute__((ext_vector_type(2)));
#define LAS __attribute__((address_space(3)))
#define GAS __attribute__((address_space(1)))

#define PACE 424
#define POFF 850
#define PRAMP 40
#define PTRANS 50
constexpr int HTOL = 150, HSTEP = 120, HMAX = 5, HCAP = 2400, HCALM = 2;
constexpr int CODE_BYTES = 11136;

constexpr int DM = 448, NQ = 4096, SK = 8192;
constexpr int UNIT_BYTES = SK * 512;
constexpr int DEAD = 0x10000000;
constexpr int AUX_NT = 2, AUX_SC1 = 16;
constexpr int RSRC3 = 0x00020000;
constexpr u32 TAG_STEP = 0x9E3779B9u;
constexpr u32 SPIN_MAX = 1u << 18;

constexpr int WS_EPOCH = 0;
constexpr int WS_SH = 1024;
constexpr int WS_ML = 4096;
constexpr int WS_QB = WS_ML + 2 * 256 * 128;
constexpr int WS_P1 = WS_QB + 16 * 8192;
constexpr int WS_P2 = WS_P1 + 16 * 65536;
constexpr int WS_END = WS_P2 + 2 * 32 * 14336;

constexpr int L_WQ = 0;
constexpr int L_QP = 32768;
constexpr int L_QC = L_QP + 2048;
constexpr int L_QF = L_QC + 176;
constexpr int WO_STRIDE = 272;
constexpr int L_WO = L_QF + 2048;
constexpr int L_REC = L_WO + 128 * WO_STRIDE;
constexpr int L_RML = L_REC + 16384;
constexpr int L_X = L_RML + 32;
constexpr int L_END = L_X + 1024;
static_assert(L_END > 81920   && (L_WO % 16) == 0 && (L_REC % 16) == 0, "LDS map");

struct Params { const void* k[2]; const void* v[2]; void* out; void* ws; const void* x[2]; const void* wq[2]; const void* wo[2]; };

__device__ __forceinline__ __amdgpu_buffer_rsrc_t mk(const void* p, int bytes) {
  return __builtin_amdgcn_make_buffer_rsrc((void*)p, 0, bytes, RSRC3);
}
__device__ __forceinline__ void st16(__amdgpu_buffer_rsrc_t r, int off, u32 v0, u32 v1, u32 tag) {
  __builtin_amdgcn_raw_buffer_store_b128(v4u{v0, tag, v1, tag}, r, off, 0, AUX_SC1);
}
__device__ __forceinline__ v4u ld16(__amdgpu_buffer_rsrc_t r, int off) { return __builtin_amdgcn_raw_buffer_load_b128(r, off, 0, AUX_SC1); }
__device__ __forceinline__ f4 mfma(v4u a, v4u b, f4 c) {
  return __builtin_amdgcn_mfma_f32_16x16x32_bf16(__builtin_bit_cast(bf8, a), __builtin_bit_cast(bf8, b), c, 0, 0, 0);
}
template <int CTRL> __device__ __forceinline__ float dppf(float x) {
  return __builtin_bit_cast(float, __builtin_amdgcn_mov_dpp(__builtin_bit_cast(int, x), CTRL, 0xf, 0xf, true));
}
template <int CTRL> __device__ __forceinline__ u32 dppu(u32 x) {
  return (u32)__builtin_amdgcn_mov_dpp((int)x, CTRL, 0xf, 0xf, true);
}
constexpr int HALF_MIRROR = 0x141, ROR4 = 0x124, ROR8 = 0x128;
__device__ __forceinline__ float fmx(float a, float b) { return __builtin_elementwise_maximum(a, b); }
__device__ __forceinline__ float x16_max(float x) {
  auto s = __builtin_amdgcn_permlane16_swap(__float_as_uint(x), __float_as_uint(x), false, false);
  return fmx(__uint_as_float(s[0]), __uint_as_float(s[1]));
}
__device__ __forceinline__ float x32_max(float x) {
  auto s = __builtin_amdgcn_permlane32_swap(__float_as_uint(x), __float_as_uint(x), false, false);
  return fmx(__uint_as_float(s[0]), __uint_as_float(s[1]));
}
__device__ __forceinline__ float x16_sum(float x) {
  auto s = __builtin_amdgcn_permlane16_swap(__float_as_uint(x), __float_as_uint(x), false, false);
  return __uint_as_float(s[0]) + __uint_as_float(s[1]);
}
__device__ __forceinline__ float x32_sum(float x) {
  auto s = __builtin_amdgcn_permlane32_swap(__float_as_uint(x), __float_as_uint(x), false, false);
  return __uint_as_float(s[0]) + __uint_as_float(s[1]);
}
__device__ __forceinline__ u32 pk_bf16(float lo, float hi) {
  f2 a = {lo, hi};
  return __builtin_bit_cast(u32, __builtin_convertvector(a, bf2));
}
__device__ __forceinline__ float bf_lo(u32 x) { return __uint_as_float(x << 16); }
__device__ __forceinline__ float bf_hi(u32 x) { return __uint_as_float(x & 0xFFFF0000u); }
__device__ __forceinline__ v4u tr_frag(LAS unsigned char* p0, LAS unsigned char* p1) {
  const s4 a = __builtin_amdgcn_ds_read_tr16_b64_v4i16((LAS s4*)p0);
  const s4 b = __builtin_amdgcn_ds_read_tr16_b64_v4i16((LAS s4*)p1);
  const v2u x = __builtin_bit_cast(v2u, a), y = __builtin_bit_cast(v2u, b);
  return v4u{x.x, x.y, y.x, y.y};
}
__device__ __forceinline__ bool any_set(u32 x) { asm("" : "+v"(x)); return __builtin_amdgcn_ballot_w64(x != 0u) != 0ull; }
#define LDSWAIT() asm volatile("s_waitcnt lgkmcnt(0)" ::: "memory")
#define SCHED() __builtin_amdgcn_sched_barrier(0)
#define FRESH() asm volatile("" ::: "memory")

__global__ void __launch_bounds__(512, 2) gqa2_decode(Params pa) {
  __shared__ __attribute__((aligned(16))) unsigned char smem[L_END];
  LAS unsigned char* L = (LAS unsigned char*)smem;
  const int bid = blockIdx.x, tid = threadIdx.x, lane = tid & 63;
  const int wave = __builtin_amdgcn_readfirstlane(tid >> 6);
  const int grp = bid >> 4, br = grp >> 3, kvh = grp & 7, jg = bid & 15;
  const int team = bid >> 3, mem = bid & 7;
  const bool helper = wave < 4;
  const int rot = team & 15;
  u64 pc0;
  asm volatile("s_getpc_b64 %0" : "=s"(pc0));
  const u32 cpf = __builtin_amdgcn_raw_buffer_load_b32(mk((const void*)(pc0 & ~127ull), CODE_BYTES), tid * 128, 0, 0);
  Params a = pa;
  const int sl = 2 * (bid & 7) + (jg >> 3);
  const int koff = ((lane >> 2) & 3) * 512 + ((lane >> 4) * 4 + (lane & 3)) * 16;
  int f_vo = helper ? (lane >> 2) * (NQ * 2) + (lane & 3) * 16 : koff;
  int so_h = 32 * wave * (NQ * 2) + (kvh * 512 + 32 * sl) * 2, so_s = team * UNIT_BYTES + (mem * 4 + wave - 4) * 8192 + rot * 262144;
  asm volatile("" : "+s"(so_h), "+s"(so_s));
  int f_so = helper ? so_h : so_s, f_s1 = helper ? 16 * NQ * 2 : 256, f_n = helper ? DM * NQ * 2 : 32 * UNIT_BYTES;
  if (tid < 11) *(LAS v4u*)(L + L_QC + tid * 16) = v4u{0u, 0u, 0u, 0u};
  asm volatile("" : "+v"(f_vo));
  asm volatile("s_waitcnt lgkmcnt(0)\n\ts_barrier" : "+s"(f_so), "+s"(f_s1), "+s"(f_n), "+s"(a.k[0]), "+s"(a.k[1]), "+s"(a.v[0]), "+s"(a.v[1]), "+s"(a.out), "+s"(a.ws),
               "+s"(a.x[0]), "+s"(a.x[1]), "+s"(a.wq[0]), "+s"(a.wq[1]), "+s"(a.wo[0]), "+s"(a.wo[1])::"memory");
  const u32 tstart = (u32)__builtin_amdgcn_s_memrealtime();
  v4u f0[8];
  {
    const __amdgpu_buffer_rsrc_t r = mk(helper ? (br ? a.wq[1] : a.wq[0]) : a.k[0], f_n);
#pragma unroll
    for (int n = 0; n < 8; ++n) f0[n] = __builtin_amdgcn_raw_buffer_load_b128(r, f_vo, f_so + ((n & 1) + (n >> 1) * 8) * f_s1, AUX_NT);
    FRESH(); SCHED();
  }
  const __amdgpu_buffer_rsrc_t wsr = mk(a.ws, WS_END);
  constexpr int F_Q = 1, F_R = 2  , F_WO = 4, F_X = 5, F_K = 6  , F_ML = 8  , W_H = 10  ;
#define FLAG_SET(g_) do { FRESH(); *(volatile LAS u32*)(L + L_QC + (g_) * 16 + (wave & 3) * 4) = 1u; FRESH(); } while (0)
#define FLAG_SUM(f_) __builtin_amdgcn_readfirstlane((int)((f_.x + f_.y) + (f_.z + f_.w)))
#define FLAG_WAIT(g_, n_, nap_, body_) do { u32 spin_ = 0; v4u f_; \
    _Pragma("unroll 1") do { if (nap_) __builtin_amdgcn_s_sleep(nap_); f_ = *(volatile LAS v4u*)(L + L_QC + (g_) * 16); FRESH(); body_; FRESH(); } \
    while (FLAG_SUM(f_) < (n_) && ++spin_ < (SPIN_MAX << 4)); } while (0)
  u32 tag;
  const int w4 = wave & 3;
#define MERGE(pb_) do { \
    f4 rc0, rc1, rc2, rc3; \
    LAS unsigned char* rcp_ = L + L_REC + (pb_) * 8192 + (tid & 127) * 16; \
    FLAG_WAIT(F_R + (pb_), 4, 0, (rc0 = *(LAS f4*)rcp_, rc1 = *(LAS f4*)(rcp_ + 2048), rc2 = *(LAS f4*)(rcp_ + 4096), rc3 = *(LAS f4*)(rcp_ + 6144))); \
    const f4 o = (rc0 + rc1) + (rc2 + rc3); \
    st16(wsr, mdst, pk_bf16(o.x, o.y), pk_bf16(o.z, o.w), tag); \
    SCHED(); } while (0)
  int mdst = w4 < 2 ? WS_P1 + ((wave >> 2) * 8 + (team & 7)) * 65536 + (((lane >> 3) & 1) * 2 + w4) * 16384 + ((team >> 3) * 8 + mem) * 512 + ((lane >> 4) * 8 + (lane & 7)) * 16 : DEAD;
#define BADTAG(r_) ((r_.y ^ tag) | (r_.w ^ tag))

  if (helper) {
    __builtin_amdgcn_s_setprio(3);
    const u32 epoch_v = __builtin_amdgcn_raw_buffer_load_b32(wsr, WS_EPOCH, 0, AUX_SC1);
    const __amdgpu_buffer_rsrc_t xr = mk(br ? a.x[1] : a.x[0], 4 * DM * 2);
    v4u xf[4];
#pragma unroll
    for (int ks = 0; ks < 4; ++ks)
      xf[ks] = __builtin_amdgcn_raw_buffer_load_b128(xr, (lane & 15) * (DM * 2) + (lane >> 4) * 16 + ks * 256, wave * 64, 0);
    const u32 touch = __builtin_amdgcn_raw_buffer_load_b32(mk(wave == 0 ? a.k[0] : wave == 1 ? a.v[0] : wave == 2 ? a.k[1] : a.v[1], 32 * UNIT_BYTES),
                                                           lane <= 2 ? team * UNIT_BYTES + min(lane << 21, UNIT_BYTES - 1024) + mem * 128 : DEAD, 0, AUX_NT);
    SCHED();
    LAS unsigned char* wl = L + L_WQ + wave * 8192;
#pragma unroll
    for (int n = 0; n < 8; ++n) *(LAS v4u*)(wl + n * 1024 + lane * 16) = f0[n];
    LDSWAIT();
    LAS unsigned char* tp = wl + ((lane >> 4) * 8 + ((lane >> 2) & 3)) * 64 + (lane & 3) * 8;
    f4 d0 = {0.f, 0.f, 0.f, 0.f}, d1 = {0.f, 0.f, 0.f, 0.f};
#pragma unroll
    for (int ks = 0; ks < 4; ++ks) {
      d0 = mfma(tr_frag(tp + ks * 2048, tp + ks * 2048 + 256), xf[ks], d0);
      d1 = mfma(tr_frag(tp + ks * 2048 + 32, tp + ks * 2048 + 32 + 256), xf[ks], d1);
    }
    asm volatile("" ::"v"(cpf));
    tag = ((u32)__builtin_amdgcn_readfirstlane((int)epoch_v) + TAG_STEP) | 1u;
    if (bid == 0 && wave == 3) __builtin_amdgcn_raw_buffer_store_b64(v2u{0u, tag}, wsr, lane < 8 ? WS_SH + lane * 256 : DEAD, 0, AUX_SC1);
    if ((lane & 15) < 4) {
      *(LAS f4*)(L + L_QP + wave * 512 + (lane & 15) * 128 + (lane >> 4) * 16) = d0;
      *(LAS f4*)(L + L_QP + wave * 512 + (lane & 15) * 128 + 64 + (lane >> 4) * 16) = d1;
    }
    LDSWAIT();
    if (wave != 0) {
      *(volatile LAS u32*)(L + L_QC + wave * 4) = 1u;
    } else {
#pragma unroll 1
      for (u32 spin = 0; spin < (SPIN_MAX << 4); ++spin) {
        const v4u f = *(volatile LAS v4u*)(L + L_QC);
        if (__builtin_amdgcn_readfirstlane((int)(f.y & f.z & f.w))) break;
      }
      FRESH();
      f2 s = *(LAS f2*)(L + L_QP + lane * 8);
#pragma unroll
      for (int w = 1; w < 4; ++w) s += *(LAS f2*)(L + L_QP + w * 512 + lane * 8);
      __builtin_amdgcn_raw_buffer_store_b64(v2u{pk_bf16(s.x, s.y), tag}, wsr, WS_QB + grp * 8192 + sl * 128 + (lane >> 4) * 2048 + (lane & 15) * 8, 0, AUX_SC1);
    }
    SCHED();
    const int qoff = WS_QB + ((wave >> 1) * 8 + (team & 7)) * 8192 + (team >> 3) * 2048 + (tid & 127) * 16;
    v4u q0;
    __builtin_amdgcn_s_sleep(40);
    {
      u32 spin = 0;
#pragma unroll 1
      do { __builtin_amdgcn_s_sleep(8); FRESH(); q0 = ld16(wsr, qoff); } while (any_set(BADTAG(q0)) && ++spin < SPIN_MAX);
    }
    *(LAS v2u*)(L + L_QF + tid * 8) = v2u{q0.x, q0.z};
    if (tid == 0) *(LAS u32*)(L + L_QC) = tag;
    __builtin_amdgcn_s_setprio(0);
    FLAG_SET(F_Q);
    asm volatile("" ::"v"(touch));
    if (wave < 2) {
      FLAG_WAIT(F_K + wave, 4, 16, (void)0);
      __builtin_amdgcn_s_sleep(120);
      f4 t[8];
#pragma unroll
      for (int n = 0; n < 8; ++n) t[n] = *(LAS f4*)(L + L_WQ + (n >> 1) * 8192 + (n & 1) * 1024 + lane * 16);
      float mx = -INFINITY, sm = 0.f;
#pragma unroll
      for (int n = 0; n < 8; ++n) mx = fmx(mx, fmx(fmx(t[n].x, t[n].y), fmx(t[n].z, t[n].w)));
      mx = fmx(mx, dppf<0x4E>(mx)); mx = fmx(mx, dppf<ROR4>(mx)); mx = x32_max(x16_max(fmx(mx, dppf<ROR8>(mx))));
#pragma unroll
      for (int n = 0; n < 8; ++n)
        sm += (__builtin_amdgcn_exp2f(t[n].x - mx) + __builtin_amdgcn_exp2f(t[n].y - mx)) + (__builtin_amdgcn_exp2f(t[n].z - mx) + __builtin_amdgcn_exp2f(t[n].w - mx));
      sm += dppf<0x4E>(sm); sm += dppf<ROR4>(sm); sm = x32_sum(x16_sum(sm + dppf<ROR8>(sm)));
      st16(wsr, lane < 8 ? WS_ML + (wave * 256 + team * 8 + mem) * 128 + lane * 16 : DEAD, __float_as_uint(mx), __float_as_uint(sm), tag);
      v4u e;
      u32 spin = 0;
#pragma unroll 1
      do { __builtin_amdgcn_s_sleep(64); FRESH(); e = ld16(wsr, WS_ML + (wave * 256 + team * 8) * 128 + lane * 16); } while (any_set(BADTAG(e)) && ++spin < SPIN_MAX);
      const float mp = __uint_as_float(e.x), M = x32_max(x16_max(fmx(mp, dppf<ROR8>(mp))));
      float ls = __builtin_amdgcn_exp2f(mp - M) * __uint_as_float(e.z);
      ls = x32_sum(x16_sum(ls + dppf<ROR8>(ls)));
      *(LAS f2*)(L + L_RML + wave * 16 + (lane & 1) * 8) = f2{M, 1.0f / ls};
      FLAG_SET(F_ML + wave);
    }

    if (wave == 3) {
      u32 spin = 0, sh = 0;
      v4u h;
#pragma unroll 1
      do {
        __builtin_amdgcn_s_sleep(24);
        FRESH();
        const v2u e = __builtin_amdgcn_raw_buffer_load_b64(wsr, WS_SH + (team & 7) * 256, 0, AUX_SC1);
        const u32 board = (u32)__builtin_amdgcn_readfirstlane((int)(e.y == tag ? e.x : 0u));
        h = *(volatile LAS v4u*)(L + L_QC + W_H * 16);
        const u32 want = (team & 7) ? 0u : (u32)__builtin_amdgcn_readfirstlane((int)h.y);
        if (want > board && want > sh && lane < 8)
          __hip_atomic_fetch_max((GAS u32*)((unsigned char*)a.ws + WS_SH + lane * 256), want, __ATOMIC_RELAXED, __HIP_MEMORY_SCOPE_AGENT);
        sh = min(max(sh, max(board, want)), (u32)HCAP);
        *(volatile LAS u32*)(L + L_QC + W_H * 16) = sh;
      } while (__builtin_amdgcn_readfirstlane((int)h.z) == 0 && sh < HCAP && ++spin < SPIN_MAX);
    }

    const int ob = bid >> 7, rb = (bid >> 2) & 31, cg = bid & 3;
    const int ci = bid & 127;
    int po = WS_P1 + (ob * 8 + (rb >> 2)) * 65536 + (rb & 3) * 16384 + wave * 4096 + lane * 16;
    int so = (cg == 3 && wave >= 2) ? DEAD : WS_P2 + (ob * 32 + rb) * 14336 + cg * 4096 + wave * 1024 + ((lane >> 3) & 1) * 512 + (lane >> 4) * 128 + (lane & 7) * 16;
    int vo = WS_P2 + ob * 32 * 14336 + ci * 128 + (lane >> 3) * 14336 + (lane & 7) * 16;
    int oo = lane < 8 ? ((lane & 3) * 896 + ob * 448 + ci * 4) * 2 + (lane >> 2) * 4 : DEAD;
    asm volatile("" : "+v"(po), "+v"(so), "+v"(vo), "+v"(oo), "+v"(mdst));
    FLAG_WAIT(F_R + 1, 1, 4, (void)0);
    MERGE(0);
    v4u wa[2][4];
    {
      v4u wo[8];
      const __amdgpu_buffer_rsrc_t wos = mk(ob ? a.wo[1] : a.wo[0], NQ * DM * 2);
#pragma unroll
      for (int n = 0; n < 8; ++n)
        wo[n] = __builtin_amdgcn_raw_buffer_load_b128(wos, (cg == 3 && (lane & 8)) ? DEAD : (lane >> 4) * (DM * 2) + (lane & 15) * 16, (rb * 128 + wave * 32 + n * 4) * (DM * 2) + cg * 256, AUX_NT);
#pragma unroll
      for (int n = 0; n < 8; ++n) *(LAS v4u*)(L + L_WO + (wave * 32 + (n & 1) * 16 + (lane >> 4) * 4 + (n >> 1)) * WO_STRIDE + (lane & 15) * 16) = wo[n];
      FLAG_SET(F_WO);
    }
    {
      v4u r[4];
#pragma unroll
      for (int n = 0; n < 4; ++n) r[n] = ld16(wsr, po + n * 1024);
      SCHED();
      FLAG_WAIT(F_WO, 4, 0, (void)0);
      LAS unsigned char* wtp = L + L_WO + ((lane >> 4) * 8 + ((lane >> 2) & 3)) * WO_STRIDE + wave * 64 + (lane & 3) * 8;
#pragma unroll
      for (int ct = 0; ct < 2; ++ct)
#pragma unroll
        for (int ks = 0; ks < 4; ++ks) wa[ct][ks] = tr_frag(wtp + ks * 32 * WO_STRIDE + ct * 32, wtp + (ks * 32 + 4) * WO_STRIDE + ct * 32);
#pragma unroll 1
      for (u32 spin = 0; spin < SPIN_MAX; ++spin) {
        u32 bad[4];
#pragma unroll
        for (int n = 0; n < 4; ++n) bad[n] = BADTAG(r[n]);
        if (!any_set((bad[0] | bad[1]) | (bad[2] | bad[3]))) break;
        FRESH();
#pragma unroll
        for (int n = 0; n < 4; ++n) if (any_set(bad[n])) r[n] = ld16(wsr, po + n * 1024);
      }
      const float v0 = (bf_lo(r[0].x) + bf_lo(r[1].x)) + (bf_lo(r[2].x) + bf_lo(r[3].x)), v1 = (bf_hi(r[0].x) + bf_hi(r[1].x)) + (bf_hi(r[2].x) + bf_hi(r[3].x));
      const float v2 = (bf_lo(r[0].z) + bf_lo(r[1].z)) + (bf_lo(r[2].z) + bf_lo(r[3].z)), v3 = (bf_hi(r[0].z) + bf_hi(r[1].z)) + (bf_hi(r[2].z) + bf_hi(r[3].z));
      const auto s0 = __builtin_amdgcn_permlane32_swap(__float_as_uint(v0), __float_as_uint(v2), false, false);
      const auto s1 = __builtin_amdgcn_permlane32_swap(__float_as_uint(v1), __float_as_uint(v3), false, false);
      *(LAS u32*)(L + L_X + wave * 256 + (lane & 31) * 8 + (lane >> 5) * 4) =
          pk_bf16(__uint_as_float(s0[0]) + __uint_as_float(s0[1]), __uint_as_float(s1[0]) + __uint_as_float(s1[1]));
      FLAG_SET(F_X);
    }
    {
      LAS unsigned char* xp = L + L_X + (lane & 3) * 256 + (lane >> 4) * 16;
      v4u x0, x1, x2, x3;
      FLAG_WAIT(F_X, 4, 0, (x0 = *(LAS v4u*)xp, x1 = *(LAS v4u*)(xp + 64), x2 = *(LAS v4u*)(xp + 128), x3 = *(LAS v4u*)(xp + 192)));
      f4 e0 = {0.f, 0.f, 0.f, 0.f}, e1 = {0.f, 0.f, 0.f, 0.f};
      e0 = mfma(wa[0][0], x0, e0); e1 = mfma(wa[1][0], x0, e1);
      e0 = mfma(wa[0][1], x1, e0); e1 = mfma(wa[1][1], x1, e1);
      e0 = mfma(wa[0][2], x2, e0); e1 = mfma(wa[1][2], x2, e1);
      e0 = mfma(wa[0][3], x3, e0); e1 = mfma(wa[1][3], x3, e1);
      const f4 ee = (lane & 8) ? e1 : e0;
      const bool hi = (lane & 4) != 0;
      st16(wsr, so, __float_as_uint(hi ? ee.z : ee.x), __float_as_uint(hi ? ee.w : ee.y), tag);
    }
    if (wave == 3 && ci < 112) {
      v4u r[4];
      __builtin_amdgcn_s_sleep(16);
      u32 spin = 0;
#pragma unroll 1
      do {
        FRESH();
#pragma unroll
        for (int n = 0; n < 4; ++n) r[n] = ld16(wsr, vo + n * 8 * 14336);
      } while (any_set((BADTAG(r[0]) | BADTAG(r[1])) | (BADTAG(r[2]) | BADTAG(r[3]))) && ++spin < SPIN_MAX);
      float y0 = (__uint_as_float(r[0].x) + __uint_as_float(r[1].x)) + (__uint_as_float(r[2].x) + __uint_as_float(r[3].x));
      float y1 = (__uint_as_float(r[0].z) + __uint_as_float(r[1].z)) + (__uint_as_float(r[2].z) + __uint_as_float(r[3].z));
      y0 += dppf<ROR8>(y0); y1 += dppf<ROR8>(y1);
      y0 = x32_sum(x16_sum(y0)); y1 = x32_sum(x16_sum(y1));
      __builtin_amdgcn_raw_buffer_store_b32(pk_bf16(y0, y1), mk(a.out, 4 * 896 * 2), oo, 0, AUX_SC1);
      if (bid == 0) __builtin_amdgcn_raw_buffer_store_b32(tag, wsr, lane == 0 ? WS_EPOCH : DEAD, 0, AUX_SC1);
    }
    __builtin_amdgcn_endpgm();
  } else {
    const int sw = wave - 4;
    u32 tnext = (tstart << 2) + POFF + ((team * 13) & 31) * PACE / 32;
    u32 shift = 0;
    int calm = 1000;
#define PACED() do { u32 now_ = (u32)__builtin_amdgcn_s_memrealtime() << 2; \
      if (--calm < 0 && (int)(now_ - tnext - shift) > HTOL) { *(volatile LAS u32*)(L + L_QC + W_H * 16 + 4) = shift + min((now_ - tnext - shift - HTOL) / (2 * HSTEP) + 1u, (u32)HMAX) * HSTEP; calm = HCALM; } \
      _Pragma("unroll 1") while ((int)(now_ - tnext - shift) < 0) { __builtin_amdgcn_s_sleep(1); now_ = (u32)__builtin_amdgcn_s_memrealtime() << 2; } \
      tnext += PACE; } while (0)
    const int voff = (lane >> 4) * 512 + (lane & 15) * 16;
    v4u d[6][8];
#define REQUEST(s_, p_, vo_, tile_) do { PACED(); const __amdgpu_buffer_rsrc_t r_ = mk(p_, 32 * UNIT_BYTES); const int so_ = so_s + ((((tile_) + rot) & 15) - rot) * 262144; \
    _Pragma("unroll") for (int i_ = 0; i_ < 8; ++i_) d[s_][i_] = __builtin_amdgcn_raw_buffer_load_b128(r_, (vo_) + ((i_ >> 1) & 1) * 2048 + (i_ & 1) * 256, so_ + (i_ >> 2) * 4096, AUX_NT); \
    SCHED(); } while (0)
#pragma unroll
    for (int n = 0; n < 8; ++n) d[2][n] = f0[n];
    REQUEST(3, a.k[0], koff, 1); tnext += PRAMP; REQUEST(4, a.k[0], koff, 2); tnext += PRAMP; REQUEST(5, a.k[0], koff, 3); tnext += PRAMP; REQUEST(0, a.k[0], koff, 4); REQUEST(1, a.k[0], koff, 5);
    asm volatile("" ::"v"(cpf));
    u32 msk[4];
#pragma unroll
    for (int v = 0; v < 4; ++v) msk[v] = (((lane & 7) >> 1) == v) ? ((lane & 1) ? 0xFFFF0000u : 0x0000FFFFu) : 0u;
    FLAG_WAIT(F_Q, 4, 0, (void)0);
    v4u qa[2], qb[2];
#define LOADQ(br_) do { _Pragma("unroll") for (int i_ = 0; i_ < 2; ++i_) { \
      const v4u f_ = *(LAS v4u*)(L + L_QF + (br_) * 1024 + ((lane >> 3) & 1) * 512 + ((lane >> 4) * 4 + (lane & 3)) * 16 + i_ * 256); \
      const v4u z_ = {0u, 0u, 0u, 0u}; qa[i_] = (lane & 4) ? z_ : f_; qb[i_] = (lane & 4) ? f_ : z_; } } while (0)
    LOADQ(0);
    tag = (u32)__builtin_amdgcn_readfirstlane((int)*(volatile LAS u32*)(L + L_QC));
    asm volatile("" : "+v"(mdst));
    constexpr float C2 = 0.0625f * 1.4426950408889634f;
    float m = -INFINITY;
    f4 acc[2] = {{0.f, 0.f, 0.f, 0.f}, {0.f, 0.f, 0.f, 0.f}};
    LAS unsigned char* scp = L + L_WQ + sw * 8192 + (lane >> 2) * 8;
#define SCORE(dst_, s_, o_) do { f4 d_ = {0.f, 0.f, 0.f, 0.f}; \
      d_ = mfma(d[s_][o_], qa[0], d_); d_ = mfma(d[s_][o_ + 1], qa[1], d_); d_ = mfma(d[s_][o_ + 2], qb[0], d_); d_ = mfma(d[s_][o_ + 3], qb[1], d_); \
      dst_ = ((dppf<0x00>(d_.x) + dppf<0x55>(d_.y)) + (dppf<0xAA>(d_.z) + dppf<0xFF>(d_.w))) * C2; } while (0)
#define PV(s_, o_, p_) do { const u32 pk_ = pk_bf16(p_, p_), po_ = dppu<HALF_MIRROR>(pk_); \
      const v4u ba_ = {pk_ & msk[0], pk_ & msk[1], po_ & msk[2], po_ & msk[3]}; \
      const v4u bb_ = {po_ & msk[0], po_ & msk[1], pk_ & msk[2], pk_ & msk[3]}; \
      acc[0] = mfma(d[s_][o_], ba_, acc[0]); acc[1] = mfma(d[s_][o_ + 1], ba_, acc[1]); \
      acc[0] = mfma(d[s_][o_ + 2], bb_, acc[0]); acc[1] = mfma(d[s_][o_ + 3], bb_, acc[1]); } while (0)
#define PIN() do { asm volatile("" : "+v"(acc[0]), "+v"(acc[1]), "+v"(m)::"memory"); SCHED(); } while (0)
#define PROCK(s_, ix_) do { float u0_, u1_; SCORE(u0_, s_, 0); SCORE(u1_, s_, 4); m = fmx(m, fmx(u0_, u1_)); \
      *(LAS f2*)(scp + (ix_) * 128) = f2{u0_, u1_}; } while (0)
#define PROCV(s_, ix_) do { const f2 u_ = *(LAS f2*)(scp + (ix_) * 128); \
      const float p0_ = __builtin_amdgcn_exp2f(u_.x - m), p1_ = __builtin_amdgcn_exp2f(u_.y - m); PV(s_, 0, p0_); PV(s_, 4, p1_); } while (0)
#define SCALE(dst_, br_) do { f2 ml_; FLAG_WAIT(F_ML + (br_), 1, 0, ml_ = *(LAS f2*)(L + L_RML + (br_) * 16 + ((lane >> 3) & 1) * 8)); \
      dst_ = __builtin_amdgcn_exp2f(m - ml_.x) * ml_.y; } while (0)
#define DUMP(br_, f_) do { \
      *(LAS f4*)(L + L_REC + ((br_) * 4 + sw) * 2048 + lane * 16) = acc[0] * (f_); \
      *(LAS f4*)(L + L_REC + ((br_) * 4 + sw) * 2048 + 1024 + lane * 16) = acc[1] * (f_); FLAG_SET(F_R + (br_)); } while (0)
    PROCK(2, 0); PIN();
    PROCK(3, 1); PIN();
    PROCK(4, 2); PIN();
    PROCK(5, 3); PIN();
#pragma unroll 1
    for (int it = 4; it < 62; it += 2) {
      const int np = (it + 2) >> 4;
      const void* pn = np == 0 ? a.k[0] : np == 1 ? a.v[0] : np == 2 ? a.k[1] : a.v[1];
      int on = (np & 1) ? voff : koff;
      asm volatile("" : "+v"(on)::"memory");
      if (it == 6) calm = 0;
      if (it == 54) *(volatile LAS u32*)(L + L_QC + W_H * 16 + 8) = 1u;
      if ((it & 15) == 14) tnext += PTRANS;
      if ((it & 15) == 0) {
        if (it & 16) {
          FLAG_SET(F_K + (it >> 5));
          m = x32_max(x16_max(fmx(m, dppf<HALF_MIRROR>(m))));
        } else {
          float f; SCALE(f, 0); DUMP(0, f); LOADQ(1);
          m = -INFINITY; acc[0] = f4{0.f, 0.f, 0.f, 0.f}; acc[1] = f4{0.f, 0.f, 0.f, 0.f};
        }
      }
#pragma unroll
      for (int s = 0; s < 2; ++s) {
        if (it & 16) PROCV(s, (it & 15) + s); else PROCK(s, (it & 15) + s);
        const u32 sh = (u32)__builtin_amdgcn_readfirstlane((int)*(volatile LAS u32*)(L + L_QC + W_H * 16));
        if (sh != shift) calm = HCALM;
        shift = sh;
        PIN();
        REQUEST(s, pn, on, ((it + 2) & 15) + s);
      }
    }
    float f;
    SCALE(f, 1);
    PROCV(0, 14); PIN();
    PROCV(1, 15); PIN();
    DUMP(1, f);
    if (sw < 2) MERGE(1);
    __builtin_amdgcn_endpgm();
  }
}

extern "C" void kernel_launch(void* const* d_in, const int* in_sizes, int n_in, void* d_out, int out_size, void* d_ws, size_t ws_size,
                              hipStream_t stream) {
  Params p;
  for (int i = 0; i < 2; ++i) {
    p.x[i] = d_in[5 * i + 0]; p.k[i] = d_in[5 * i + 1]; p.v[i] = d_in[5 * i + 2]; p.wq[i] = d_in[5 * i + 3]; p.wo[i] = d_in[5 * i + 4];
  }
  p.out = d_out; p.ws = d_ws;
  hipLaunchKernelGGL(gqa2_decode, dim3(256), dim3(512), 0, stream, p);
}
```

```cpp
#include <hip/hip_runtime.h>
#include <hip/hip_bf16.h>

typedef unsigned u32;
typedef unsigned long long u64;
typedef u32 v4u __attribute__((ext_vector_type(4)));
typedef u32 v2u __attribute__((ext_vector_type(2)));
typedef float f4 __attribute__((ext_vector_type(4)));
typedef float f2 __attribute__((ext_vector_type(2)));
typedef short s4 __attribute__((ext_vector_type(4)));
typedef __bf16 bf8 __attribute__((ext_vector_type(8)));
typedef __bf16 bf2 __attribute__((ext_vector_type(2)));
#define LAS __attribute__((address_space(3)))
#define GAS __attribute__((address_space(1)))

#define PACE 426
#define POFF 830
#define PRAMP 40
#define PTRANS 20
#define GDEL 890
#define H2D 13
constexpr int HTOL = 150, HSTEP = 120, HMAX = 5, HCAP = 2400, HCALM = 2;
constexpr int HJUMP = 1200;
constexpr int CODE_BYTES = 11520;

constexpr int DM = 448, NQ = 4096, SK = 8192;
constexpr int UNIT_BYTES = SK * 512;
constexpr int DEAD = 0x10000000;
constexpr int AUX_NT = 2, AUX_SC1 = 16;
constexpr int RSRC3 = 0x00020000;
constexpr u32 TAG_STEP = 0x9E3779B9u;
constexpr u32 SPIN_MAX = 1u << 18;

constexpr int WS_EPOCH = 0;
constexpr int WS_SH = 1024;
constexpr int WS_ML = 4096;
constexpr int WS_QB = WS_ML + 2 * 256 * 128;
constexpr int WS_P1 = WS_QB + 16 * 8192;
constexpr int WS_P2 = WS_P1 + 16 * 65536;
constexpr int WS_END = WS_P2 + 2 * 32 * 14336;

constexpr int L_WQ = 0;
constexpr int L_QP = 32768;
constexpr int L_QC = L_QP + 2048;
constexpr int L_QF = L_QC + 176;
constexpr int WO_STRIDE = 272;
constexpr int L_WO = L_QF + 2048;
constexpr int L_REC = L_WO + 128 * WO_STRIDE;
constexpr int L_RML = L_REC + 16384;
constexpr int L_X = L_RML + 32;
constexpr int L_END = L_X + 1024;
static_assert(L_END > 81920   && (L_WO % 16) == 0 && (L_REC % 16) == 0, "LDS map");

struct Params { const void* k[2]; const void* v[2]; void* out; void* ws; const void* x[2]; const void* wq[2]; const void* wo[2]; };

__device__ __forceinline__ __amdgpu_buffer_rsrc_t mk(const void* p, int bytes) {
  return __builtin_amdgcn_make_buffer_rsrc((void*)p, 0, bytes, RSRC3);
}
__device__ __forceinline__ void st16(__amdgpu_buffer_rsrc_t r, int off, u32 v0, u32 v1, u32 tag) {
  __builtin_amdgcn_raw_buffer_store_b128(v4u{v0, tag, v1, tag}, r, off, 0, AUX_SC1);
}
__device__ __forceinline__ v4u ld16(__amdgpu_buffer_rsrc_t r, int off) { return __builtin_amdgcn_raw_buffer_load_b128(r, off, 0, AUX_SC1); }
__device__ __forceinline__ f4 mfma(v4u a, v4u b, f4 c) {
  return __builtin_amdgcn_mfma_f32_16x16x32_bf16(__builtin_bit_cast(bf8, a), __builtin_bit_cast(bf8, b), c, 0, 0, 0);
}
template <int CTRL> __device__ __forceinline__ float dppf(float x) {
  return __builtin_bit_cast(float, __builtin_amdgcn_mov_dpp(__builtin_bit_cast(int, x), CTRL, 0xf, 0xf, true));
}
template <int CTRL> __device__ __forceinline__ u32 dppu(u32 x) {
  return (u32)__builtin_amdgcn_mov_dpp((int)x, CTRL, 0xf, 0xf, true);
}
constexpr int HALF_MIRROR = 0x141, ROR4 = 0x124, ROR8 = 0x128;
__device__ __forceinline__ float fmx(float a, float b) { return __builtin_elementwise_maximum(a, b); }
__device__ __forceinline__ float x16_max(float x) {
  auto s = __builtin_amdgcn_permlane16_swap(__float_as_uint(x), __float_as_uint(x), false, false);
  return fmx(__uint_as_float(s[0]), __uint_as_float(s[1]));
}
__device__ __forceinline__ float x32_max(float x) {
  auto s = __builtin_amdgcn_permlane32_swap(__float_as_uint(x), __float_as_uint(x), false, false);
  return fmx(__uint_as_float(s[0]), __uint_as_float(s[1]));
}
__device__ __forceinline__ float x16_sum(float x) {
  auto s = __builtin_amdgcn_permlane16_swap(__float_as_uint(x), __float_as_uint(x), false, false);
  return __uint_as_float(s[0]) + __uint_as_float(s[1]);
}
__device__ __forceinline__ float x32_sum(float x) {
  auto s = __builtin_amdgcn_permlane32_swap(__float_as_uint(x), __float_as_uint(x), false, false);
  return __uint_as_float(s[0]) + __uint_as_float(s[1]);
}
__device__ __forceinline__ u32 pk_bf16(float lo, float hi) {
  f2 a = {lo, hi};
  return __builtin_bit_cast(u32, __builtin_convertvector(a, bf2));
}
__device__ __forceinline__ float bf_lo(u32 x) { return __uint_as_float(x << 16); }
__device__ __forceinline__ float bf_hi(u32 x) { return __uint_as_float(x & 0xFFFF0000u); }
__device__ __forceinline__ v4u tr_frag(LAS unsigned char* p0, LAS unsigned char* p1) {
  const s4 a = __builtin_amdgcn_ds_read_tr16_b64_v4i16((LAS s4*)p0);
  const s4 b = __builtin_amdgcn_ds_read_tr16_b64_v4i16((LAS s4*)p1);
  const v2u x = __builtin_bit_cast(v2u, a), y = __builtin_bit_cast(v2u, b);
  return v4u{x.x, x.y, y.x, y.y};
}
__device__ __forceinline__ bool any_set(u32 x) { asm("" : "+v"(x)); return __builtin_amdgcn_ballot_w64(x != 0u) != 0ull; }
#define LDSWAIT() asm volatile("s_waitcnt lgkmcnt(0)" ::: "memory")
#define SCHED() __builtin_amdgcn_sched_barrier(0)
#define FRESH() asm volatile("" ::: "memory")

__global__ void __launch_bounds__(512, 2) gqa2_decode(Params pa) {
  __shared__ __attribute__((aligned(16))) unsigned char smem[L_END];
  LAS unsigned char* L = (LAS unsigned char*)smem;
  const int bid = blockIdx.x, tid = threadIdx.x, lane = tid & 63;
  const int wave = __builtin_amdgcn_readfirstlane(tid >> 6);
  const int grp = bid >> 4, br = grp >> 3, kvh = grp & 7, jg = bid & 15;
  const int team = bid >> 3, mem = bid & 7;
  const bool helper = wave < 4;
  const int rot = team & 15;
  u64 pc0;
  asm volatile("s_getpc_b64 %0" : "=s"(pc0));
  const u32 cpf = __builtin_amdgcn_raw_buffer_load_b32(mk((const void*)(pc0 & ~127ull), CODE_BYTES), tid * 128, 0, 0);
  Params a = pa;
  const int sl = 2 * (bid & 7) + (jg >> 3);
  const int koff = ((lane >> 2) & 3) * 512 + ((lane >> 4) * 4 + (lane & 3)) * 16;
  int f_vo = helper ? (lane >> 2) * (NQ * 2) + (lane & 3) * 16 : koff;
  int so_h = 32 * wave * (NQ * 2) + (kvh * 512 + 32 * sl) * 2, so_s = team * UNIT_BYTES + (mem * 4 + wave - 4) * 8192 + rot * 262144;
  asm volatile("" : "+s"(so_h), "+s"(so_s));
  int f_so = helper ? so_h : so_s, f_s1 = helper ? 16 * NQ * 2 : 256, f_n = helper ? DM * NQ * 2 : 32 * UNIT_BYTES;
  if (tid < 11) *(LAS v4u*)(L + L_QC + tid * 16) = v4u{0u, 0u, 0u, 0u};
  asm volatile("" : "+v"(f_vo));
  asm volatile("s_waitcnt lgkmcnt(0)\n\ts_barrier" : "+s"(f_so), "+s"(f_s1), "+s"(f_n), "+s"(a.k[0]), "+s"(a.k[1]), "+s"(a.v[0]), "+s"(a.v[1]), "+s"(a.out), "+s"(a.ws),
               "+s"(a.x[0]), "+s"(a.x[1]), "+s"(a.wq[0]), "+s"(a.wq[1]), "+s"(a.wo[0]), "+s"(a.wo[1])::"memory");
  const u32 tstart = (u32)__builtin_amdgcn_s_memrealtime();
  v4u f0[8];
  {
    const __amdgpu_buffer_rsrc_t r = mk(helper ? (br ? a.wq[1] : a.wq[0]) : a.k[0], f_n);
#pragma unroll
    for (int n = 0; n < 8; ++n) f0[n] = __builtin_amdgcn_raw_buffer_load_b128(r, f_vo, f_so + ((n & 1) + (n >> 1) * 8) * f_s1, AUX_NT);
    FRESH(); SCHED();
  }
  const __amdgpu_buffer_rsrc_t wsr = mk(a.ws, WS_END);
  constexpr int F_Q = 1, F_R = 2  , F_WO = 4, F_X = 5, F_K = 6  , F_ML = 8  , W_H = 10  ;
#define FLAG_SET(g_) do { FRESH(); *(volatile LAS u32*)(L + L_QC + (g_) * 16 + (wave & 3) * 4) = 1u; FRESH(); } while (0)
#define FLAG_SUM(f_) __builtin_amdgcn_readfirstlane((int)((f_.x + f_.y) + (f_.z + f_.w)))
#define FLAG_WAIT(g_, n_, nap_, body_) do { u32 spin_ = 0; v4u f_; \
    _Pragma("unroll 1") do { if (nap_) __builtin_amdgcn_s_sleep(nap_); f_ = *(volatile LAS v4u*)(L + L_QC + (g_) * 16); FRESH(); body_; FRESH(); } \
    while (FLAG_SUM(f_) < (n_) && ++spin_ < (SPIN_MAX << 4)); } while (0)
  u32 tag;
  const int w4 = wave & 3;
#define MERGE(pb_) do { \
    f4 rc0, rc1, rc2, rc3; \
    LAS unsigned char* rcp_ = L + L_REC + (pb_) * 8192 + (tid & 127) * 16; \
    FLAG_WAIT(F_R + (pb_), 4, 0, (rc0 = *(LAS f4*)rcp_, rc1 = *(LAS f4*)(rcp_ + 2048), rc2 = *(LAS f4*)(rcp_ + 4096), rc3 = *(LAS f4*)(rcp_ + 6144))); \
    const f4 o = (rc0 + rc1) + (rc2 + rc3); \
    st16(wsr, mdst, pk_bf16(o.x, o.y), pk_bf16(o.z, o.w), tag); \
    SCHED(); } while (0)
  int mdst = w4 < 2 ? WS_P1 + ((wave >> 2) * 8 + (team & 7)) * 65536 + (((lane >> 3) & 1) * 2 + w4) * 16384 + ((team >> 3) * 8 + mem) * 512 + ((lane >> 4) * 8 + (lane & 7)) * 16 : DEAD;
#define BADTAG(r_) ((r_.y ^ tag) | (r_.w ^ tag))

  if (helper) {
    __builtin_amdgcn_s_setprio(3);
    const u32 epoch_v = __builtin_amdgcn_raw_buffer_load_b32(wsr, WS_EPOCH, 0, AUX_SC1);
    const __amdgpu_buffer_rsrc_t xr = mk(br ? a.x[1] : a.x[0], 4 * DM * 2);
    v4u xf[4];
#pragma unroll
    for (int ks = 0; ks < 4; ++ks)
      xf[ks] = __builtin_amdgcn_raw_buffer_load_b128(xr, (lane & 15) * (DM * 2) + (lane >> 4) * 16 + ks * 256, wave * 64, 0);
    const u32 touch = __builtin_amdgcn_raw_buffer_load_b32(mk(wave == 0 ? a.k[0] : wave == 1 ? a.v[0] : wave == 2 ? a.k[1] : a.v[1], 32 * UNIT_BYTES),
                                                           lane <= 2 ? team * UNIT_BYTES + min(lane << 21, UNIT_BYTES - 1024) + mem * 128 : DEAD, 0, AUX_NT);
    SCHED();
    LAS unsigned char* wl = L + L_WQ + wave * 8192;
#pragma unroll
    for (int n = 0; n < 8; ++n) *(LAS v4u*)(wl + n * 1024 + lane * 16) = f0[n];
    LDSWAIT();
    LAS unsigned char* tp = wl + ((lane >> 4) * 8 + ((lane >> 2) & 3)) * 64 + (lane & 3) * 8;
    f4 d0 = {0.f, 0.f, 0.f, 0.f}, d1 = {0.f, 0.f, 0.f, 0.f};
#pragma unroll
    for (int ks = 0; ks < 4; ++ks) {
      d0 = mfma(tr_frag(tp + ks * 2048, tp + ks * 2048 + 256), xf[ks], d0);
      d1 = mfma(tr_frag(tp + ks * 2048 + 32, tp + ks * 2048 + 32 + 256), xf[ks], d1);
    }
    asm volatile("" ::"v"(cpf));
    tag = ((u32)__builtin_amdgcn_readfirstlane((int)epoch_v) + TAG_STEP) | 1u;
    if (bid == 0 && wave == 3) __builtin_amdgcn_raw_buffer_store_b64(v2u{0u, tag}, wsr, lane < 8 ? WS_SH + lane * 256 : DEAD, 0, AUX_SC1);
    if ((lane & 15) < 4) {
      *(LAS f4*)(L + L_QP + wave * 512 + (lane & 15) * 128 + (lane >> 4) * 16) = d0;
      *(LAS f4*)(L + L_QP + wave * 512 + (lane & 15) * 128 + 64 + (lane >> 4) * 16) = d1;
    }
    LDSWAIT();
    if (wave != 0) {
      *(volatile LAS u32*)(L + L_QC + wave * 4) = 1u;
    } else {
#pragma unroll 1
      for (u32 spin = 0; spin < (SPIN_MAX << 4); ++spin) {
        const v4u f = *(volatile LAS v4u*)(L + L_QC);
        if (__builtin_amdgcn_readfirstlane((int)(f.y & f.z & f.w))) break;
      }
      FRESH();
      f2 s = *(LAS f2*)(L + L_QP + lane * 8);
#pragma unroll
      for (int w = 1; w < 4; ++w) s += *(LAS f2*)(L + L_QP + w * 512 + lane * 8);
      __builtin_amdgcn_raw_buffer_store_b64(v2u{pk_bf16(s.x, s.y), tag}, wsr, WS_QB + grp * 8192 + sl * 128 + (lane >> 4) * 2048 + (lane & 15) * 8, 0, AUX_SC1);
    }
    SCHED();
    const int qoff = WS_QB + ((wave >> 1) * 8 + (team & 7)) * 8192 + (team >> 3) * 2048 + (tid & 127) * 16;
    v4u q0;
    __builtin_amdgcn_s_sleep(40);
    {
      u32 spin = 0;
#pragma unroll 1
      do { __builtin_amdgcn_s_sleep(8); FRESH(); q0 = ld16(wsr, qoff); } while (any_set(BADTAG(q0)) && ++spin < SPIN_MAX);
    }
    *(LAS v2u*)(L + L_QF + tid * 8) = v2u{q0.x, q0.z};
    if (tid == 0) *(LAS u32*)(L + L_QC) = tag;
    __builtin_amdgcn_s_setprio(0);
    FLAG_SET(F_Q);
    asm volatile("" ::"v"(touch));
    if (wave < 2) {
      FLAG_WAIT(F_K + wave, 4, 16, (void)0);
      __builtin_amdgcn_s_sleep(120);
      f4 t[8];
#pragma unroll
      for (int n = 0; n < 8; ++n) t[n] = *(LAS f4*)(L + L_WQ + (n >> 1) * 8192 + (n & 1) * 1024 + lane * 16);
      float mx = -INFINITY, sm = 0.f;
#pragma unroll
      for (int n = 0; n < 8; ++n) mx = fmx(mx, fmx(fmx(t[n].x, t[n].y), fmx(t[n].z, t[n].w)));
      mx = fmx(mx, dppf<0x4E>(mx)); mx = fmx(mx, dppf<ROR4>(mx)); mx = x32_max(x16_max(fmx(mx, dppf<ROR8>(mx))));
#pragma unroll
      for (int n = 0; n < 8; ++n)
        sm += (__builtin_amdgcn_exp2f(t[n].x - mx) + __builtin_amdgcn_exp2f(t[n].y - mx)) + (__builtin_amdgcn_exp2f(t[n].z - mx) + __builtin_amdgcn_exp2f(t[n].w - mx));
      sm += dppf<0x4E>(sm); sm += dppf<ROR4>(sm); sm = x32_sum(x16_sum(sm + dppf<ROR8>(sm)));
      st16(wsr, lane < 8 ? WS_ML + (wave * 256 + team * 8 + mem) * 128 + lane * 16 : DEAD, __float_as_uint(mx), __float_as_uint(sm), tag);
      v4u e;
      u32 spin = 0;
#pragma unroll 1
      do { __builtin_amdgcn_s_sleep(64); FRESH(); e = ld16(wsr, WS_ML + (wave * 256 + team * 8) * 128 + lane * 16); } while (any_set(BADTAG(e)) && ++spin < SPIN_MAX);
      const float mp = __uint_as_float(e.x), M = x32_max(x16_max(fmx(mp, dppf<ROR8>(mp))));
      float ls = __builtin_amdgcn_exp2f(mp - M) * __uint_as_float(e.z);
      ls = x32_sum(x16_sum(ls + dppf<ROR8>(ls)));
      *(LAS f2*)(L + L_RML + wave * 16 + (lane & 1) * 8) = f2{M, 1.0f / ls};
      FLAG_SET(F_ML + wave);
    }

    if (wave == 3) {
      u32 spin = 0, code = 0, sh = 0, base = 0, tev = tstart, tch = tstart + 800u;
      int s0 = 2 * HSTEP;
      v4u h;
#pragma unroll 1
      do {
        __builtin_amdgcn_s_sleep(24);
        FRESH();
        const v2u e = __builtin_amdgcn_raw_buffer_load_b64(wsr, WS_SH + (team & 7) * 256, 0, AUX_SC1);
        const u32 board = (u32)__builtin_amdgcn_readfirstlane((int)(e.y == tag ? e.x : 0u));
        h = *(volatile LAS v4u*)(L + L_QC + W_H * 16);
        const u32 now = (u32)__builtin_amdgcn_s_memrealtime();
        if ((int)(now - tch) > 800) { base = code; tev = now; }
        const u32 wabs = (team & 7) ? 0u : (u32)__builtin_amdgcn_readfirstlane((int)h.y);
        u32 want = 0;
        if (wabs > sh && (code & 4095u) < HCAP) want = code + (wabs - sh);
        if (want >= base + 3 * HSTEP && want < base + 4096u && (int)(now - tev) < 500) want = base + 4096u;
        if (want > board && want > code && lane < 8)
          __hip_atomic_fetch_max((GAS u32*)((unsigned char*)a.ws + WS_SH + lane * 256), want, __ATOMIC_RELAXED, __HIP_MEMORY_SCOPE_AGENT);
        const u32 nc = min(max(code, max(board, want)), 4u * 4096u - 1u);
        if (nc != code) tch = now;
        code = nc;
        sh = max(sh, (code >> 12) * HJUMP + (code & 4095u));
        if ((int)(now - tstart) < 2600) s0 = (code & 4095u) + 2 * HSTEP;
        *(volatile LAS u32*)(L + L_QC + W_H * 16) = sh | ((u32)max((int)(code & 4095u) - s0, 0) / 32u) << 16;
      } while (__builtin_amdgcn_readfirstlane((int)h.z) == 0 && ++spin < SPIN_MAX);
    }

    const int ob = bid >> 7, rb = (bid >> 2) & 31, cg = bid & 3;
    const int ci = bid & 127;
    int po = WS_P1 + (ob * 8 + (rb >> 2)) * 65536 + (rb & 3) * 16384 + wave * 4096 + lane * 16;
    int so = (cg == 3 && wave >= 2) ? DEAD : WS_P2 + (ob * 32 + rb) * 14336 + cg * 4096 + wave * 1024 + ((lane >> 3) & 1) * 512 + (lane >> 4) * 128 + (lane & 7) * 16;
    int vo = WS_P2 + ob * 32 * 14336 + ci * 128 + (lane >> 3) * 14336 + (lane & 7) * 16;
    int oo = lane < 8 ? ((lane & 3) * 896 + ob * 448 + ci * 4) * 2 + (lane >> 2) * 4 : DEAD;
    asm volatile("" : "+v"(po), "+v"(so), "+v"(vo), "+v"(oo), "+v"(mdst));
    FLAG_WAIT(F_R + 1, 1, 4, (void)0);
    MERGE(0);
    v4u wa[2][4];
    v4u r[4];
    {
      v4u wo[8];
      const __amdgpu_buffer_rsrc_t wos = mk(ob ? a.wo[1] : a.wo[0], NQ * DM * 2);
#pragma unroll
      for (int n = 0; n < 8; ++n)
        wo[n] = __builtin_amdgcn_raw_buffer_load_b128(wos, (cg == 3 && (lane & 8)) ? DEAD : (lane >> 4) * (DM * 2) + (lane & 15) * 16, (rb * 128 + wave * 32 + n * 4) * (DM * 2) + cg * 256, AUX_NT);
      SCHED();
      const u32 tg = GDEL + (u32)__builtin_amdgcn_readfirstlane((int)*(volatile LAS u32*)(L + L_QC + W_H * 16 + 12));
#pragma unroll 1
      while ((int)(((u32)__builtin_amdgcn_s_memrealtime() << 2) - tg) < 0) __builtin_amdgcn_s_sleep(1);
#pragma unroll
      for (int n = 0; n < 4; ++n) r[n] = ld16(wsr, po + n * 1024);
      SCHED();
#pragma unroll
      for (int n = 0; n < 8; ++n) *(LAS v4u*)(L + L_WO + (wave * 32 + (n & 1) * 16 + (lane >> 4) * 4 + (n >> 1)) * WO_STRIDE + (lane & 15) * 16) = wo[n];
      FLAG_SET(F_WO);
    }
    {
      FLAG_WAIT(F_WO, 4, 0, (void)0);
      LAS unsigned char* wtp = L + L_WO + ((lane >> 4) * 8 + ((lane >> 2) & 3)) * WO_STRIDE + wave * 64 + (lane & 3) * 8;
#pragma unroll
      for (int ct = 0; ct < 2; ++ct)
#pragma unroll
        for (int ks = 0; ks < 4; ++ks) wa[ct][ks] = tr_frag(wtp + ks * 32 * WO_STRIDE + ct * 32, wtp + (ks * 32 + 4) * WO_STRIDE + ct * 32);
#pragma unroll 1
      for (u32 spin = 0; spin < SPIN_MAX; ++spin) {
        u32 bad[4];
#pragma unroll
        for (int n = 0; n < 4; ++n) bad[n] = BADTAG(r[n]);
        if (!any_set((bad[0] | bad[1]) | (bad[2] | bad[3]))) break;
        FRESH();
#pragma unroll
        for (int n = 0; n < 4; ++n) if (any_set(bad[n])) r[n] = ld16(wsr, po + n * 1024);
      }
      const float v0 = (bf_lo(r[0].x) + bf_lo(r[1].x)) + (bf_lo(r[2].x) + bf_lo(r[3].x)), v1 = (bf_hi(r[0].x) + bf_hi(r[1].x)) + (bf_hi(r[2].x) + bf_hi(r[3].x));
      const float v2 = (bf_lo(r[0].z) + bf_lo(r[1].z)) + (bf_lo(r[2].z) + bf_lo(r[3].z)), v3 = (bf_hi(r[0].z) + bf_hi(r[1].z)) + (bf_hi(r[2].z) + bf_hi(r[3].z));
      const auto s0 = __builtin_amdgcn_permlane32_swap(__float_as_uint(v0), __float_as_uint(v2), false, false);
      const auto s1 = __builtin_amdgcn_permlane32_swap(__float_as_uint(v1), __float_as_uint(v3), false, false);
      *(LAS u32*)(L + L_X + wave * 256 + (lane & 31) * 8 + (lane >> 5) * 4) =
          pk_bf16(__uint_as_float(s0[0]) + __uint_as_float(s0[1]), __uint_as_float(s1[0]) + __uint_as_float(s1[1]));
      FLAG_SET(F_X);
    }
    {
      LAS unsigned char* xp = L + L_X + (lane & 3) * 256 + (lane >> 4) * 16;
      v4u x0, x1, x2, x3;
      FLAG_WAIT(F_X, 4, 0, (x0 = *(LAS v4u*)xp, x1 = *(LAS v4u*)(xp + 64), x2 = *(LAS v4u*)(xp + 128), x3 = *(LAS v4u*)(xp + 192)));
      f4 e0 = {0.f, 0.f, 0.f, 0.f}, e1 = {0.f, 0.f, 0.f, 0.f};
      e0 = mfma(wa[0][0], x0, e0); e1 = mfma(wa[1][0], x0, e1);
      e0 = mfma(wa[0][1], x1, e0); e1 = mfma(wa[1][1], x1, e1);
      e0 = mfma(wa[0][2], x2, e0); e1 = mfma(wa[1][2], x2, e1);
      e0 = mfma(wa[0][3], x3, e0); e1 = mfma(wa[1][3], x3, e1);
      const f4 ee = (lane & 8) ? e1 : e0;
      const bool hi = (lane & 4) != 0;
      st16(wsr, so, __float_as_uint(hi ? ee.z : ee.x), __float_as_uint(hi ? ee.w : ee.y), tag);
    }
    if (wave == 3 && ci < 112) {
      __builtin_amdgcn_s_sleep(H2D);
      u32 spin = 0;
#pragma unroll 1
      do {
        FRESH();
#pragma unroll
        for (int n = 0; n < 4; ++n) r[n] = ld16(wsr, vo + n * 8 * 14336);
      } while (any_set((BADTAG(r[0]) | BADTAG(r[1])) | (BADTAG(r[2]) | BADTAG(r[3]))) && ++spin < SPIN_MAX);
      float y0 = (__uint_as_float(r[0].x) + __uint_as_float(r[1].x)) + (__uint_as_float(r[2].x) + __uint_as_float(r[3].x));
      float y1 = (__uint_as_float(r[0].z) + __uint_as_float(r[1].z)) + (__uint_as_float(r[2].z) + __uint_as_float(r[3].z));
      y0 += dppf<ROR8>(y0); y1 += dppf<ROR8>(y1);
      y0 = x32_sum(x16_sum(y0)); y1 = x32_sum(x16_sum(y1));
      __builtin_amdgcn_raw_buffer_store_b32(pk_bf16(y0, y1), mk(a.out, 4 * 896 * 2), oo, 0, AUX_SC1);
      if (bid == 0) __builtin_amdgcn_raw_buffer_store_b32(tag, wsr, lane == 0 ? WS_EPOCH : DEAD, 0, AUX_SC1);
    }
    __builtin_amdgcn_endpgm();
  } else {
    const int sw = wave - 4;
    const u32 phase = ((team * 13) & 31) * PACE / 32;
    u32 tnext = (tstart << 2) + POFF + phase;
    u32 shift = 0, period = PACE;
    int calm = 1000;
#define PACED() do { u32 now_ = (u32)__builtin_amdgcn_s_memrealtime() << 2; \
      if (--calm < 0 && (int)(now_ - tnext - shift) > HTOL) { *(volatile LAS u32*)(L + L_QC + W_H * 16 + 4) = shift + min((now_ - tnext - shift - HTOL) / (2 * HSTEP) + 1u, (u32)HMAX) * HSTEP; calm = HCALM; } \
      _Pragma("unroll 1") while ((int)(now_ - tnext - shift) < 0) { __builtin_amdgcn_s_sleep(1); now_ = (u32)__builtin_amdgcn_s_memrealtime() << 2; } \
      tnext += period; } while (0)
    const int voff = (lane >> 4) * 512 + (lane & 15) * 16;
    v4u d[6][8];
#define REQUEST(s_, p_, vo_, tile_) do { PACED(); const __amdgpu_buffer_rsrc_t r_ = mk(p_, 32 * UNIT_BYTES); const int so_ = so_s + ((((tile_) + rot) & 15) - rot) * 262144; \
    _Pragma("unroll") for (int i_ = 0; i_ < 8; ++i_) d[s_][i_] = __builtin_amdgcn_raw_buffer_load_b128(r_, (vo_) + ((i_ >> 1) & 1) * 2048 + (i_ & 1) * 256, so_ + (i_ >> 2) * 4096, AUX_NT); \
    SCHED(); } while (0)
#pragma unroll
    for (int n = 0; n < 8; ++n) d[2][n] = f0[n];
    REQUEST(3, a.k[0], koff, 1); tnext += PRAMP; REQUEST(4, a.k[0], koff, 2); tnext += PRAMP; REQUEST(5, a.k[0], koff, 3); tnext += PRAMP; REQUEST(0, a.k[0], koff, 4); REQUEST(1, a.k[0], koff, 5);
    asm volatile("" ::"v"(cpf));
    u32 msk[4];
#pragma unroll
    for (int v = 0; v < 4; ++v) msk[v] = (((lane & 7) >> 1) == v) ? ((lane & 1) ? 0xFFFF0000u : 0x0000FFFFu) : 0u;
    FLAG_WAIT(F_Q, 4, 0, (void)0);
    v4u qa[2], qb[2];
#define LOADQ(br_) do { _Pragma("unroll") for (int i_ = 0; i_ < 2; ++i_) { \
      const v4u f_ = *(LAS v4u*)(L + L_QF + (br_) * 1024 + ((lane >> 3) & 1) * 512 + ((lane >> 4) * 4 + (lane & 3)) * 16 + i_ * 256); \
      const v4u z_ = {0u, 0u, 0u, 0u}; qa[i_] = (lane & 4) ? z_ : f_; qb[i_] = (lane & 4) ? f_ : z_; } } while (0)
    LOADQ(0);
    tag = (u32)__builtin_amdgcn_readfirstlane((int)*(volatile LAS u32*)(L + L_QC));
    asm volatile("" : "+v"(mdst));
    constexpr float C2 = 0.0625f * 1.4426950408889634f;
    float m = -INFINITY;
    f4 acc[2] = {{0.f, 0.f, 0.f, 0.f}, {0.f, 0.f, 0.f, 0.f}};
    LAS unsigned char* scp = L + L_WQ + sw * 8192 + (lane >> 2) * 8;
#define SCORE(dst_, s_, o_) do { f4 d_ = {0.f, 0.f, 0.f, 0.f}; \
      d_ = mfma(d[s_][o_], qa[0], d_); d_ = mfma(d[s_][o_ + 1], qa[1], d_); d_ = mfma(d[s_][o_ + 2], qb[0], d_); d_ = mfma(d[s_][o_ + 3], qb[1], d_); \
      dst_ = ((dppf<0x00>(d_.x) + dppf<0x55>(d_.y)) + (dppf<0xAA>(d_.z) + dppf<0xFF>(d_.w))) * C2; } while (0)
#define PV(s_, o_, p_) do { const u32 pk_ = pk_bf16(p_, p_), po_ = dppu<HALF_MIRROR>(pk_); \
      const v4u ba_ = {pk_ & msk[0], pk_ & msk[1], po_ & msk[2], po_ & msk[3]}; \
      const v4u bb_ = {po_ & msk[0], po_ & msk[1], pk_ & msk[2], pk_ & msk[3]}; \
      acc[0] = mfma(d[s_][o_], ba_, acc[0]); acc[1] = mfma(d[s_][o_ + 1], ba_, acc[1]); \
      acc[0] = mfma(d[s_][o_ + 2], bb_, acc[0]); acc[1] = mfma(d[s_][o_ + 3], bb_, acc[1]); } while (0)
#define PIN() do { asm volatile("" : "+v"(acc[0]), "+v"(acc[1]), "+v"(m)::"memory"); SCHED(); } while (0)
#define PROCK(s_, ix_) do { float u0_, u1_; SCORE(u0_, s_, 0); SCORE(u1_, s_, 4); m = fmx(m, fmx(u0_, u1_)); \
      *(LAS f2*)(scp + (ix_) * 128) = f2{u0_, u1_}; } while (0)
#define PROCV(s_, ix_) do { const f2 u_ = *(LAS f2*)(scp + (ix_) * 128); \
      const float p0_ = __builtin_amdgcn_exp2f(u_.x - m), p1_ = __builtin_amdgcn_exp2f(u_.y - m); PV(s_, 0, p0_); PV(s_, 4, p1_); } while (0)
#define SCALE(dst_, br_) do { f2 ml_; FLAG_WAIT(F_ML + (br_), 1, 0, ml_ = *(LAS f2*)(L + L_RML + (br_) * 16 + ((lane >> 3) & 1) * 8)); \
      dst_ = __builtin_amdgcn_exp2f(m - ml_.x) * ml_.y; } while (0)
#define DUMP(br_, f_) do { \
      *(LAS f4*)(L + L_REC + ((br_) * 4 + sw) * 2048 + lane * 16) = acc[0] * (f_); \
      *(LAS f4*)(L + L_REC + ((br_) * 4 + sw) * 2048 + 1024 + lane * 16) = acc[1] * (f_); FLAG_SET(F_R + (br_)); } while (0)
    PROCK(2, 0); PIN();
    PROCK(3, 1); PIN();
    PROCK(4, 2); PIN();
    PROCK(5, 3); PIN();
#pragma unroll 1
    for (int it = 4; it < 62; it += 2) {
      const int np = (it + 2) >> 4;
      const void* pn = np == 0 ? a.k[0] : np == 1 ? a.v[0] : np == 2 ? a.k[1] : a.v[1];
      int on = (np & 1) ? voff : koff;
      asm volatile("" : "+v"(on)::"memory");
      if (it == 6) calm = 0;
      if (it == 58) *(volatile LAS u32*)(L + L_QC + W_H * 16 + 8) = 1u;
      if ((it & 15) == 14) tnext += PTRANS;
      if ((it & 15) == 0) {
        if (it & 16) {
          FLAG_SET(F_K + (it >> 5));
          m = x32_max(x16_max(fmx(m, dppf<HALF_MIRROR>(m))));
        } else {
          float f; SCALE(f, 0); DUMP(0, f); LOADQ(1);
          m = -INFINITY; acc[0] = f4{0.f, 0.f, 0.f, 0.f}; acc[1] = f4{0.f, 0.f, 0.f, 0.f};
        }
      }
#pragma unroll
      for (int s = 0; s < 2; ++s) {
        if (it & 16) PROCV(s, (it & 15) + s); else PROCK(s, (it & 15) + s);
        const u32 hw = (u32)__builtin_amdgcn_readfirstlane((int)*(volatile LAS u32*)(L + L_QC + W_H * 16));
        if ((hw & 0xFFFFu) != shift) calm = HCALM;
        shift = hw & 0xFFFFu; period = PACE + (hw >> 16);
        PIN();
        REQUEST(s, pn, on, ((it + 2) & 15) + s);
      }
    }
    *(volatile LAS u32*)(L + L_QC + W_H * 16 + 12) = tnext + shift - phase;
    float f;
    SCALE(f, 1);
    PROCV(0, 14); PIN();
    PROCV(1, 15); PIN();
    DUMP(1, f);
    if (sw < 2) MERGE(1);
    __builtin_amdgcn_endpgm();
  }
}

extern "C" void kernel_launch(void* const* d_in, const int* in_sizes, int n_in, void* d_out, int out_size, void* d_ws, size_t ws_size,
                              hipStream_t stream) {
  Params p;
  for (int i = 0; i < 2; ++i) {
    p.x[i] = d_in[5 * i + 0]; p.k[i] = d_in[5 * i + 1]; p.v[i] = d_in[5 * i + 2]; p.wq[i] = d_in[5 * i + 3]; p.wo[i] = d_in[5 * i + 4];
  }
  p.out = d_out; p.ws = d_ws;
  hipLaunchKernelGGL(gqa2_decode, dim3(256), dim3(512), 0, stream, p);
}
```
